```python
import torch

def _softmax(x, axis=-1):
    return torch.softmax(x, dim=axis)
def _gelu(x):
    return torch.nn.functional.gelu(x, approximate="tanh")
def _silu(x):
    return torch.nn.functional.silu(x)
def _sigmoid(x):
    return torch.sigmoid(x)
def _layernorm(x, axis=-1, eps=1e-5):
    m = x.mean(dim=axis, keepdim=True)
    v = x.var(dim=axis, keepdim=True, unbiased=False)
    return (x - m) / torch.sqrt(v + eps)
def _scatter_add(tgt, idx, src, axis=0):
    return tgt.clone().index_add_(axis, idx, src)
def _segment_sum(src, seg, n):
    out = torch.zeros((n,) + src.shape[1:], dtype=src.dtype)
    return out.index_add_(0, seg, src)
def _pad(x, pw):
    flat = tuple(v for pair in reversed(pw) for v in pair)
    return torch.nn.functional.pad(x, flat)

kernel_name = 'sequential_preLN__sliding_block_cached__swiglu_ffn__decode'

def setup_inputs():

    _w = (lambda a: (lambda t: t.to(torch.bfloat16) if t.is_floating_point() else t)(torch.as_tensor(a)))
    def _A_setup():
        B, Sq, D = 4, 1, 448
        Hq, Hkv, Sk, Dh = 16, 8, 4096, 256
        assert Hq % Hkv == 0, (Hq, Hkv)
        return {
            "x": _w((lambda *s: torch.randn(*s, dtype=torch.bfloat16) * 0.05)(B, Sq, D)),
            "k_cache": _w((lambda *s: torch.randn(*s, dtype=torch.bfloat16) * 3.2)(B, Hkv, Sk, Dh)),
            "v_cache": _w((lambda *s: torch.randn(*s, dtype=torch.bfloat16) * 0.4)(B, Hkv, Sk, Dh)),
            "wq": _w((lambda *s: torch.randn(*s, dtype=torch.bfloat16) * 3.2)(D, Hq * Dh) * (D ** -0.5)),
            "wo": _w((lambda *s: torch.randn(*s, dtype=torch.bfloat16) * 0.4)(Hq * Dh, D) * ((Hq * Dh) ** -0.5)),
        }
    _A_in = _A_setup()

    def _B_setup():
        B, S, D, F = 4, 1, 448, 1792
        return {
            "x": _w((lambda *s: torch.randn(*s, dtype=torch.bfloat16) * 0.05)(B, S, D)),
            "w_gate": _w((lambda *s: torch.randn(*s, dtype=torch.bfloat16) * 0.05)(D, F)),
            "w_up": _w((lambda *s: torch.randn(*s, dtype=torch.bfloat16) * 0.05)(D, F)),
            "w_down": _w((lambda *s: torch.randn(*s, dtype=torch.bfloat16) * 0.05)(F, D)),
        }
    _B_in = _B_setup()

    return {
        "A_x": _A_in["x"],
        "A_k_cache": _A_in["k_cache"],
        "A_v_cache": _A_in["v_cache"],
        "A_wq": _A_in["wq"],
        "A_wo": _A_in["wo"],
        "B_x": _B_in["x"],
        "B_w_gate": _B_in["w_gate"],
        "B_w_up": _B_in["w_up"],
        "B_w_down": _B_in["w_down"],
    }

def reference(A_x, A_k_cache, A_v_cache, A_wq, A_wo, B_x, B_w_gate, B_w_up, B_w_down):
    def _A_ref(x, k_cache, v_cache, wq, wo):
        B, Sq, D = x.shape
        Hq, Dh = 16, 256
        Hkv, Sk = k_cache.shape[1], k_cache.shape[2]
        W = min(4096, max(256, Sk // 4))
        rep = Hq // Hkv
        q = (lambda x, *d: x.permute(*d))((x @ wq).reshape(B, Sq, Hq, Dh), 0, 2, 1, 3)
        k = (lambda x, n, axis: torch.repeat_interleave(x, n, dim=axis))(k_cache[:, :, Sk - W:, :], rep, axis=1)
        v = (lambda x, n, axis: torch.repeat_interleave(x, n, dim=axis))(v_cache[:, :, Sk - W:, :], rep, axis=1)
        scores = torch.einsum("bhqd,bhkd->bhqk", q, k) * (Dh ** -0.5)
        p = _softmax(scores, axis=-1)
        o = torch.einsum("bhqk,bhkd->bhqd", p, v)
        return (lambda x, *d: x.permute(*d))(o, 0, 2, 1, 3).reshape(B, Sq, Hq * Dh) @ wo

    def _B_ref(x, w_gate, w_up, w_down):
        g = _silu(x @ w_gate)
        u = x @ w_up
        return (g * u) @ w_down

    x = A_x
    h = x + _A_ref(_layernorm(x, axis=-1), A_k_cache, A_v_cache, A_wq, A_wo)
    h2 = h + _B_ref(_layernorm(h, axis=-1), B_w_gate, B_w_up, B_w_down)
    return h2
```

```cpp
#include <hip/hip_runtime.h>
#include <hip/hip_bf16.h>
#include <stdint.h>

typedef unsigned short u16;
__device__ __forceinline__ float bf2f(u16 b) { return __uint_as_float(((unsigned)b) << 16); }
__device__ __forceinline__ u16 f2bf(float f) { unsigned u = __float_as_uint(f); u += 0x7FFFu + ((u >> 16) & 1u); return (u16)(u >> 16); }
__device__ __forceinline__ float rbf(float f) { return bf2f(f2bf(f)); }

constexpr int B = 4, D = 448, HQ = 16, HKV = 8, SK = 4096, DH = 256, W = 1024, F = 1792;

__device__ float block_sum(float v, float* red) {
  for (int o = 32; o > 0; o >>= 1) v += __shfl_xor(v, o);
  int w = threadIdx.x >> 6, nw = blockDim.x >> 6;
  __syncthreads();
  if ((threadIdx.x & 63) == 0) red[w] = v;
  __syncthreads();
  float s = 0; for (int i = 0; i < nw; i++) s += red[i];
  return s;
}
__device__ float block_max(float v, float* red) {
  for (int o = 32; o > 0; o >>= 1) v = fmaxf(v, __shfl_xor(v, o));
  int w = threadIdx.x >> 6, nw = blockDim.x >> 6;
  __syncthreads();
  if ((threadIdx.x & 63) == 0) red[w] = v;
  __syncthreads();
  float s = -INFINITY; for (int i = 0; i < nw; i++) s = fmaxf(s, red[i]);
  return s;
}
__device__ void ln_row(const float* in, float* out, float* red) {
  float v = 0; for (int i = threadIdx.x; i < D; i += blockDim.x) v += in[i];
  float mean = block_sum(v, red) / D;
  float mb = rbf(mean);
  float s = 0; for (int i = threadIdx.x; i < D; i += blockDim.x) { float d = in[i] - mean; s += d * d; }
  float var = rbf(block_sum(s, red) / D);
  float den = rbf(sqrtf(rbf(var + 1e-5f)));
  for (int i = threadIdx.x; i < D; i += blockDim.x) out[i] = rbf(rbf(in[i] - mb) / den);
  __syncthreads();
}

__global__ void k_q(const u16* x, const u16* wq, float* q) {
  __shared__ float xin[D], xs[D], red[16];
  int b = blockIdx.y;
  for (int i = threadIdx.x; i < D; i += 256) xin[i] = bf2f(x[b * D + i]);
  __syncthreads();
  ln_row(xin, xs, red);
  int c = blockIdx.x * 256 + threadIdx.x;
  float a = 0;
  for (int k = 0; k < D; k++) a += xs[k] * bf2f(wq[(size_t)k * 4096 + c]);
  q[b * 4096 + c] = rbf(a);
}
__global__ void k_attn(const float* q, const u16* K, const u16* V, float* o) {
  __shared__ float qs[DH], p[W], red[16];
  int h = blockIdx.x, b = blockIdx.y, kvh = h / 2;
  qs[threadIdx.x] = q[b * 4096 + h * DH + threadIdx.x];
  __syncthreads();
  const u16* Kb = K + ((size_t)(b * HKV + kvh) * SK + (SK - W)) * DH;
  const u16* Vb = V + ((size_t)(b * HKV + kvh) * SK + (SK - W)) * DH;
  float mx = -INFINITY;
  for (int j = threadIdx.x; j < W; j += 256) {
    float a = 0;
    for (int d = 0; d < DH; d++) a += qs[d] * bf2f(Kb[(size_t)j * DH + d]);
    a = rbf(rbf(a) * 0.0625f);
    p[j] = a; mx = fmaxf(mx, a);
  }
  mx = block_max(mx, red);
  float s = 0;
  for (int j = threadIdx.x; j < W; j += 256) { float e = expf(p[j] - mx); p[j] = e; s += e; }
  s = block_sum(s, red);
  for (int j = threadIdx.x; j < W; j += 256) p[j] = rbf(p[j] / s);
  __syncthreads();
  float a = 0;
  for (int j = 0; j < W; j++) a += p[j] * bf2f(Vb[(size_t)j * DH + threadIdx.x]);
  o[b * 4096 + h * DH + threadIdx.x] = rbf(a);
}
__global__ void k_wo(const float* o, const u16* wo, const u16* x, float* h) {
  int b = blockIdx.y, c = blockIdx.x * 64 + threadIdx.x;
  float a = 0;
  for (int k = 0; k < 4096; k++) a += o[b * 4096 + k] * bf2f(wo[(size_t)k * D + c]);
  h[b * D + c] = rbf(bf2f(x[b * D + c]) + rbf(a));
}
__global__ void k_gu(const float* h, const u16* wg, const u16* wu, float* act) {
  __shared__ float xin[D], xs[D], red[16];
  int b = blockIdx.y;
  for (int i = threadIdx.x; i < D; i += 256) xin[i] = h[b * D + i];
  __syncthreads();
  ln_row(xin, xs, red);
  int c = blockIdx.x * 256 + threadIdx.x;
  float g = 0, u = 0;
  for (int k = 0; k < D; k++) { g += xs[k] * bf2f(wg[(size_t)k * F + c]); u += xs[k] * bf2f(wu[(size_t)k * F + c]); }
  g = rbf(g); u = rbf(u);
  float sg = rbf(g / (1.f + expf(-g)));
  act[b * F + c] = rbf(sg * u);
}
__global__ void k_dn(const float* act, const u16* wd, const float* h, u16* out) {
  int b = blockIdx.y, c = blockIdx.x * 64 + threadIdx.x;
  float a = 0;
  for (int k = 0; k < F; k++) a += act[b * F + k] * bf2f(wd[(size_t)k * D + c]);
  out[b * D + c] = f2bf(h[b * D + c] + rbf(a));
}

extern "C" void kernel_launch(void* const* d_in, const int* in_sizes, int n_in, void* d_out, int out_size, void* d_ws, size_t ws_size, hipStream_t stream) {
  const u16* x = (const u16*)d_in[0]; const u16* K = (const u16*)d_in[1]; const u16* V = (const u16*)d_in[2];
  const u16* wq = (const u16*)d_in[3]; const u16* wo = (const u16*)d_in[4];
  const u16* wg = (const u16*)d_in[6]; const u16* wu = (const u16*)d_in[7]; const u16* wd = (const u16*)d_in[8];
  float* ws = (float*)d_ws;
  float* q = ws; float* o = ws + 16384; float* h = ws + 32768; float* act = ws + 36864;
  k_q<<<dim3(16, B), 256, 0, stream>>>(x, wq, q);
  k_attn<<<dim3(HQ, B), 256, 0, stream>>>(q, K, V, o);
  k_wo<<<dim3(7, B), 64, 0, stream>>>(o, wo, x, h);
  k_gu<<<dim3(7, B), 256, 0, stream>>>(h, wg, wu, act);
  k_dn<<<dim3(7, B), 64, 0, stream>>>(act, wd, h, (u16*)d_out);
}
```

```cpp
#include <hip/hip_runtime.h>
#include <stdint.h>

#define LAS __attribute__((address_space(3)))
typedef unsigned v4u __attribute__((ext_vector_type(4)));
typedef unsigned v2u __attribute__((ext_vector_type(2)));
typedef float v4f __attribute__((ext_vector_type(4)));
typedef float v2f __attribute__((ext_vector_type(2)));
typedef __bf16 v8b __attribute__((ext_vector_type(8)));
typedef __bf16 v2b __attribute__((ext_vector_type(2)));
typedef unsigned long long u64;
typedef __amdgpu_buffer_rsrc_t rsrc_t;

#define AUXKV 2
#define AUXW 2

namespace fk {
constexpr int NT_KV = AUXKV, NT_W = AUXW, SC1 = 16;
constexpr unsigned WS_EPOCH = 0, WS_FLAG = 4096, WS_ST = 8192, WS_HP = 16384, WS_QG = 131072, WS_OP = 655360, WS_FP = 1179648, WS_END = 1581056;
constexpr unsigned WS_STAMP = 2u << 20;
constexpr unsigned L_FXS = 0, L_FQ = 16, L_FMAX = 32, L_FP = 48, L_FVI = 64, L_E = 80, L_CNT = 88, L_WMAX = 96, L_WSUM = 128;
constexpr unsigned L_XS = 256, L_Q = L_XS + 3584, L_P = L_Q + 1024, L_RED = L_P + 512 + 256, L_STAT = L_RED + 8192, L_HB = L_STAT + 256, L_END = L_HB + 3584;
constexpr unsigned LDS_BYTES = 96 * 1024;
constexpr unsigned SENT = 0xFFFFFFFFu;
constexpr unsigned SPIN_MAX = 1u << 17;

__device__ __forceinline__ rsrc_t mk(const void* p, unsigned bytes) { return __builtin_amdgcn_make_buffer_rsrc((void*)p, 0, bytes, 0x00020000); }
__device__ __forceinline__ unsigned pk(float a, float b) { v2b v = {(__bf16)a, (__bf16)b}; return __builtin_bit_cast(unsigned, v); }
__device__ __forceinline__ float lo_f(unsigned u) { return __uint_as_float(u << 16); }
__device__ __forceinline__ float hi_f(unsigned u) { return __uint_as_float(u & 0xFFFF0000u); }
__device__ __forceinline__ float rbf(float a) { return lo_f(pk(a, a)); }
__device__ __forceinline__ v4f mfma(v4u a, v4u b, v4f c) { return __builtin_amdgcn_mfma_f32_16x16x32_bf16(__builtin_bit_cast(v8b, a), __builtin_bit_cast(v8b, b), c, 0, 0, 0); }
template <int C> __device__ __forceinline__ unsigned dppu(unsigned x) { return (unsigned)__builtin_amdgcn_mov_dpp((int)x, C, 0xf, 0xf, true); }
template <int C> __device__ __forceinline__ float dppf(float x) { return __uint_as_float(dppu<C>(__float_as_uint(x))); }
constexpr int XOR1 = 0xB1, XOR2 = 0x4E, ROR4 = 0x124, ROR8 = 0x128;
__device__ __forceinline__ float x16_sum(float x) {
  auto s = __builtin_amdgcn_permlane16_swap(__float_as_uint(x), __float_as_uint(x), false, false);
  x = __uint_as_float(s[0]) + __uint_as_float(s[1]);
  auto t = __builtin_amdgcn_permlane32_swap(__float_as_uint(x), __float_as_uint(x), false, false);
  return __uint_as_float(t[0]) + __uint_as_float(t[1]);
}
__device__ __forceinline__ float x16_max(float x) {
  auto s = __builtin_amdgcn_permlane16_swap(__float_as_uint(x), __float_as_uint(x), false, false);
  x = fmaxf(__uint_as_float(s[0]), __uint_as_float(s[1]));
  auto t = __builtin_amdgcn_permlane32_swap(__float_as_uint(x), __float_as_uint(x), false, false);
  return fmaxf(__uint_as_float(t[0]), __uint_as_float(t[1]));
}
__device__ __forceinline__ unsigned prm(unsigned hi, unsigned lo, int odd) { return __builtin_amdgcn_perm(hi, lo, odd ? 0x07060302u : 0x05040100u); }
#define CBAR() asm volatile("" ::: "memory")
#define SB() __builtin_amdgcn_sched_barrier(0)
template <int N> __device__ __forceinline__ void waitvm() { asm volatile("s_waitcnt vmcnt(%0)" ::"n"(N) : "memory"); }
__device__ __forceinline__ void wg_barrier() { SB(); asm volatile("s_waitcnt lgkmcnt(0)\n\ts_barrier" ::: "memory"); SB(); }
#define PIN4(a, b, c, d) asm volatile("" : "+v"(a), "+v"(b), "+v"(c), "+v"(d))
__shared__ __attribute__((aligned(16))) char smem[LDS_BYTES];
template <class T> __device__ __forceinline__ LAS T* lds(unsigned off) { return (LAS T*)((LAS char*)smem + off); }
__device__ __forceinline__ void set_flag(unsigned off, int i) { CBAR(); *(volatile LAS unsigned*)lds<unsigned>(off + 4 * i) = 1u; CBAR(); }
__device__ __forceinline__ void wait_flags(unsigned off) {
  _Pragma("unroll 1") for (unsigned n = 0; n < (SPIN_MAX << 6); ++n) {
    v4u f = *(volatile LAS v4u*)lds<v4u>(off);
    if (__builtin_amdgcn_readfirstlane(f.x & f.y & f.z & f.w)) break;
    __builtin_amdgcn_s_sleep(1);
  }
  CBAR();
}
__device__ __forceinline__ bool has_sent(v4u v) { return max(max(v.x, v.y), max(v.z, v.w)) == SENT; }
__device__ __forceinline__ v4u mks(const void* p, unsigned bytes) { const u64 a = (u64)p; return v4u{(unsigned)a, (unsigned)(a >> 32) & 0xFFFFu, bytes, 0x00020000u}; }
template <int N, int STRIDE> __device__ __forceinline__ void poll_issue(v4u rs, unsigned off, v4u (&v)[N]) {
#pragma unroll
  for (int i = 0; i < N; i++) asm volatile("buffer_load_dwordx4 %0, %1, %2, %3 offen sc1" : "=v"(v[i]) : "v"(off), "s"(rs), "s"(i * STRIDE) : "memory");
}
template <int N> __device__ __forceinline__ void poll_wait(v4u (&v)[N]) {
  if constexpr (N == 8) asm volatile("s_waitcnt vmcnt(0)" : "+v"(v[0]), "+v"(v[1]), "+v"(v[2]), "+v"(v[3]), "+v"(v[4]), "+v"(v[5]), "+v"(v[6]), "+v"(v[7])::"memory");
  else asm volatile("s_waitcnt vmcnt(0)" : "+v"(v[0]), "+v"(v[1]), "+v"(v[2]), "+v"(v[3]), "+v"(v[4]), "+v"(v[5]), "+v"(v[6])::"memory");
}
template <int N, int STRIDE> __device__ __forceinline__ void poll_finish(v4u rs, unsigned off, v4u (&v)[N]) {
#pragma unroll 1
  for (unsigned n = 0; n < SPIN_MAX; ++n) {
    poll_wait<N>(v);
    u64 m[N], any = 0;
#pragma unroll
    for (int i = 0; i < N; i++) { m[i] = __builtin_amdgcn_ballot_w64(has_sent(v[i])); any |= m[i]; }
    if (any == 0) break;
#pragma unroll
    for (int i = 0; i < N; i++)
      asm volatile("s_mov_b64 exec, %4\n\tbuffer_load_dwordx4 %0, %1, %2, %3 offen sc1\n\ts_mov_b64 exec, -1" : "+v"(v[i]) : "v"(off), "s"(rs), "s"(i * STRIDE), "s"(m[i]) : "memory");
  }
  poll_wait<N>(v);
}
#define ST(i)
#define ST_FLUSH()

__device__ __forceinline__ v4f fold8(const v4f (&acc)[8], bool kk) {
  v4f o;
#pragma unroll
  for (int i = 0; i < 4; i++) {
    float a0 = acc[i][0], a1 = acc[i][1], b0 = acc[i + 4][0], b1 = acc[i + 4][1];
    asm("" : "+v"(a0), "+v"(a1), "+v"(b0), "+v"(b1));
    const float send = kk ? a1 : b0, keep = kk ? b1 : a0;
    o[i] = keep + dppf<ROR8>(send);
  }
  return o;
}

__global__ void __launch_bounds__(768) fused_block(const char* __restrict__ x, const char* __restrict__ Kc, const char* __restrict__ Vc,
                                                   const char* __restrict__ wq, const char* __restrict__ wo, const char* __restrict__ wg,
                                                   const char* __restrict__ wu, const char* __restrict__ wd, char* __restrict__ out, char* ws) {
  const int lane = threadIdx.x & 63, w = __builtin_amdgcn_readfirstlane(threadIdx.x >> 6);
  const int g = blockIdx.x & 7, r = blockIdx.x >> 3, b = r >> 3, s = r & 7;
  const int rot = s + 2 * g + 8 * b;
  asm volatile("" ::"s"(x), "s"(Kc), "s"(Vc), "s"(wq), "s"(wo), "s"(wg), "s"(wu), "s"(wd), "s"(out), "s"(ws));
  const rsrc_t rws = mk(ws, WS_END);
  const v4u sws = mks(ws, WS_END);
  unsigned e_lo, e_hi;

  if (w >= 8) {
    const int dq = w - 8;
    const rsrc_t rv = mk(Vc + ((size_t)(b * 8 + g) * 4096 + 3072 + s * 128) * 512, 65536);
    const unsigned vo = (lane >> 3) * 512 + dq * 128 + (lane & 7) * 16;
    const v4u xv = __builtin_amdgcn_raw_buffer_load_b128(mk(x + dq * 896, 896), lane * 16, 0, 0);
    if (w == 11 && lane < 24) *lds<unsigned>(lane * 4) = 0u;
    __builtin_amdgcn_s_setprio(3);
    wg_barrier();
    {
      float e[8];
#pragma unroll
      for (int i = 0; i < 4; i++) { e[2 * i] = lo_f(xv[i]); e[2 * i + 1] = hi_f(xv[i]); }
      float sum = ((e[0] + e[1]) + (e[2] + e[3])) + ((e[4] + e[5]) + (e[6] + e[7]));
      sum += dppf<XOR1>(sum); sum += dppf<XOR2>(sum); sum += dppf<ROR4>(sum); sum += dppf<ROR8>(sum); sum = x16_sum(sum);
      const float mean = sum * (1.f / 448.f);
      float ss = 0.f;
#pragma unroll
      for (int i = 0; i < 8; i++) { const float d = e[i] - mean; ss = fmaf(d, d, ss); }
      if (lane >= 56) ss = 0.f;
      ss += dppf<XOR1>(ss); ss += dppf<XOR2>(ss); ss += dppf<ROR4>(ss); ss += dppf<ROR8>(ss); ss = x16_sum(ss);
      const float mb = rbf(mean), den = rbf(__builtin_amdgcn_sqrtf(rbf(rbf(ss * (1.f / 448.f)) + 1e-5f))), ri = __builtin_amdgcn_rcpf(den);
      v4u o;
#pragma unroll
      for (int i = 0; i < 4; i++) { const unsigned d = pk(e[2 * i] - mb, e[2 * i + 1] - mb); o[i] = pk(lo_f(d) * ri, hi_f(d) * ri); }
      if (lane < 56) *lds<v4u>(L_XS + dq * 896 + lane * 16) = o;
      set_flag(L_FXS, dq);
    }
    ST(5);
    __builtin_amdgcn_s_setprio(1);
    v4u vf[16];
#pragma unroll
    for (int n = 0; n < 16; n++) { vf[n] = __builtin_amdgcn_raw_buffer_load_b128(rv, vo, ((n + rot) & 15) * 4096, NT_KV); SB(); }
    set_flag(L_FVI, dq);
    ST(1);
    __builtin_amdgcn_s_setprio(3);
    wait_flags(L_FP);
    ST(2);
    const unsigned pa_off = L_P + (((lane >> 2) & 1) * 8 + (lane >> 4) * 2 + (lane & 1)) * 32;
    const v4u pa0 = *lds<v4u>(pa_off), pa1 = *lds<v4u>(pa_off + 16);
    v4f acc[8];
#pragma unroll
    for (int i = 0; i < 8; i++) {
      v4u bb = {prm(vf[1][i >> 1], vf[0][i >> 1], i & 1), prm(vf[3][i >> 1], vf[2][i >> 1], i & 1), prm(vf[5][i >> 1], vf[4][i >> 1], i & 1), prm(vf[7][i >> 1], vf[6][i >> 1], i & 1)};
      acc[i] = mfma(pa0, bb, v4f{0.f, 0.f, 0.f, 0.f});
    }
    SB();
    v4u bb[8];
#pragma unroll
    for (int j = 0; j < 4; j++) {
#pragma unroll
      for (int i = 0; i < 8; i++) bb[i][j] = prm(vf[9 + 2 * j][i >> 1], vf[8 + 2 * j][i >> 1], i & 1);
      SB();
    }
    ST(3);
#pragma unroll
    for (int i = 0; i < 8; i++) acc[i] = mfma(pa1, bb[i], acc[i]);
    const v4f o = fold8(acc, (lane >> 3) & 1);
    if (lane < 32) {
      const unsigned off = WS_OP + (((((g * 2 + (lane >> 4)) * 4 + dq) * 8 + s) * 4 + b) * 256) + (lane & 7) * 32 + ((lane >> 3) & 1) * 16;
      __builtin_amdgcn_raw_buffer_store_b128(__builtin_bit_cast(v4u, o), rws, off, 0, SC1);
    }
    ST(4);
    ST_FLUSH();
    return;
  }
  if (w >= 4) {
    const int kw = w - 4;
    const rsrc_t rk = mk(Kc + ((size_t)(b * 8 + g) * 4096 + 3072 + s * 128) * 512, 65536);
    const unsigned vo = ((lane & 15) >> 1) * 512 + (lane >> 4) * 32 + (lane & 1) * 16;
    __builtin_amdgcn_s_setprio(2);
    v4u kf[16];
#pragma unroll
    for (int n = 0; n < 16; n++) {
      kf[n] = __builtin_amdgcn_raw_buffer_load_b128(rk, vo + (n & 3) * 128, (((n >> 2) * 4 + kw + rot) & 15) * 4096, NT_KV);
      SB();
      if (n == 3) wg_barrier();
    }
    ST(1);
    wait_flags(L_FQ);
    ST(2);
    { const v2u e = *lds<v2u>(L_E); e_lo = __builtin_amdgcn_readfirstlane(e.x); e_hi = __builtin_amdgcn_readfirstlane(e.y); }
    const unsigned q_off = L_Q + ((lane >> 1) & 1) * 512 + (lane >> 4) * 32 + (lane & 1) * 16;
    v4u qf[4];
#pragma unroll
    for (int c = 0; c < 4; c++) qf[c] = *lds<v4u>(q_off + c * 128);
    float sc[4];
#pragma unroll
    for (int u = 0; u < 4; u++) {
      v4f a = {0.f, 0.f, 0.f, 0.f};
#pragma unroll
      for (int c = 0; c < 4; c++) a = mfma(kf[u * 4 + c], qf[c], a);
      const bool hh = lane & 1;
      float a0 = a[0], a1 = a[1], a2 = a[2], a3 = a[3];
      asm("" : "+v"(a0), "+v"(a1), "+v"(a2), "+v"(a3));
      sc[u] = (hh ? a3 : a0) + dppf<XOR1>(hh ? a1 : a2);
    }
    ST(3);
    const int uu = (lane >> 2) & 3;
    PIN4(sc[0], sc[1], sc[2], sc[3]);
    float sv = uu == 0 ? sc[0] : uu == 1 ? sc[1] : uu == 2 ? sc[2] : sc[3];
    sv = rbf(rbf(sv) * 0.0625f);
    float m = sv;
    m = fmaxf(m, dppf<XOR1>(m)); m = fmaxf(m, dppf<ROR4>(m)); m = fmaxf(m, dppf<ROR8>(m)); m = x16_max(m);
    const int head = (lane >> 1) & 1;
    if (lane < 4 && !(lane & 1)) *lds<float>(L_WMAX + head * 16 + kw * 4) = m;
    set_flag(L_FMAX, kw);
    wait_flags(L_FMAX);
    { const v4f mm = *lds<v4f>(L_WMAX + head * 16); m = fmaxf(fmaxf(mm[0], mm[1]), fmaxf(mm[2], mm[3])); }
    const float p = __expf(sv - m);
    *lds<unsigned short>(L_P + (head * 8 + (lane >> 4) * 2 + (lane & 1)) * 32 + (uu * 4 + kw) * 2) = (unsigned short)pk(p, p);
    float l = p;
    l += dppf<XOR1>(l); l += dppf<ROR4>(l); l += dppf<ROR8>(l); l = x16_sum(l);
    if (lane < 4 && !(lane & 1)) *lds<float>(L_WSUM + head * 16 + kw * 4) = l;
    set_flag(L_FP, kw);
    if (kw == 0) {
      wait_flags(L_FP);
      const v4f ll = *lds<v4f>(L_WSUM + head * 16);
      l = (ll[0] + ll[1]) + (ll[2] + ll[3]);
      if (lane < 4 && !(lane & 1)) __builtin_amdgcn_raw_buffer_store_b64(v2u{__float_as_uint(m), __float_as_uint(l)}, rws, WS_ST + g * 512 + b * 128 + s * 16 + head * 8, 0, SC1);
    }
    ST(4);
  } else {
    __builtin_amdgcn_s_setprio(3);
    const v2u ev = __builtin_amdgcn_raw_buffer_load_b64(rws, WS_EPOCH, 0, SC1);
    v4u wf[14];
    const int cb = r & 7, kq = r >> 3;
    if (w == 0) {
      const rsrc_t rq = mk(wq + (size_t)kq * 112 * 8192 + (g * 512 + cb * 64) * 2, 111 * 8192 + 128);
      const unsigned vo = (lane >> 3) * 14 * 8192 + (lane & 7) * 16;
#pragma unroll
      for (int n = 0; n < 14; n++) { wf[n] = __builtin_amdgcn_raw_buffer_load_b128(rq, vo, n * 8192, NT_W); SB(); }
    }
    wg_barrier();
    if (w == 1) {
      const v4u sent = {SENT, SENT, SENT, SENT};
#pragma unroll
      for (int mth = 0; mth < 2; mth++)
        __builtin_amdgcn_raw_buffer_store_b128(sent, rws, WS_OP + ((((g * 8 + mth * 4 + (lane >> 4)) * 8 + s) * 4 + b) * 256) + (lane & 15) * 16, 0, SC1);
      if (lane == 0) __builtin_amdgcn_raw_buffer_store_b128(sent, rws, WS_ST + g * 512 + b * 128 + s * 16, 0, SC1);
      if (r < 14) { if (lane < 32) __builtin_amdgcn_raw_buffer_store_b128(sent, rws, WS_HP + (g * 14 + r) * 512 + lane * 16, 0, SC1); }
      else if (r < 21) {
#pragma unroll
        for (int i = 0; i < 7; i++) __builtin_amdgcn_raw_buffer_store_b128(sent, rws, WS_FP + (g * 7 + r - 14) * 7168 + i * 1024 + lane * 16, 0, SC1);
      }
    }
    ST(1);
    { const u64 e1 = (((u64)ev.y << 32) | ev.x) + 1; e_lo = __builtin_amdgcn_readfirstlane((unsigned)e1); e_hi = __builtin_amdgcn_readfirstlane((unsigned)(e1 >> 32)); }
    const unsigned tag8 = e_hi & 0xFFu;
    ST(2);
    if (w == 0) {
      wait_flags(L_FXS);
      const unsigned xo = L_XS + ((lane >> 2) & 3) * 896 + kq * 224 + ((lane >> 4) * 2 + (lane & 1)) * 28;
      unsigned xa[7];
#pragma unroll
      for (int i = 0; i < 7; i++) xa[i] = *lds<unsigned>(xo + 4 * i);
      const v4u a0 = {xa[0], xa[1], xa[2], xa[3]}, a1 = {xa[4], xa[5], xa[6], 0u};
      v4f acc[8];
#pragma unroll
      for (int i = 0; i < 8; i++) {
        const v4u b0 = {prm(wf[1][i >> 1], wf[0][i >> 1], i & 1), prm(wf[3][i >> 1], wf[2][i >> 1], i & 1), prm(wf[5][i >> 1], wf[4][i >> 1], i & 1), prm(wf[7][i >> 1], wf[6][i >> 1], i & 1)};
        const v4u b1 = {prm(wf[9][i >> 1], wf[8][i >> 1], i & 1), prm(wf[11][i >> 1], wf[10][i >> 1], i & 1), prm(wf[13][i >> 1], wf[12][i >> 1], i & 1), 0u};
        acc[i] = mfma(a1, b1, mfma(a0, b0, v4f{0.f, 0.f, 0.f, 0.f}));
      }
      const v4f o = fold8(acc, (lane >> 3) & 1);
      unsigned gl[4];
#pragma unroll
      for (int i = 0; i < 4; i++) gl[i] = ((__float_as_uint(o[i]) + 0x80u) & 0xFFFFFF00u) | tag8;
      const unsigned off = WS_QG + (((g * 4 + (lane >> 4)) * 4 + kq) * 512 + cb * 64 + (lane & 7) * 8 + ((lane >> 3) & 1) * 4) * 8;
      __builtin_amdgcn_raw_buffer_store_b128(v4u{gl[0], e_lo, gl[1], e_lo}, rws, off, 0, SC1);
      __builtin_amdgcn_raw_buffer_store_b128(v4u{gl[2], e_lo, gl[3], e_lo}, rws, off + 16, 0, SC1);
      *lds<v2u>(L_E) = v2u{e_lo, e_hi};
      CBAR(); *(volatile LAS unsigned*)lds<unsigned>(L_CNT) = 1u; CBAR();
    }
    if (w == 1) {
      waitvm<0>();
      if (lane == 0) __builtin_amdgcn_raw_buffer_store_b64(v2u{e_lo, e_hi}, rws, WS_FLAG + blockIdx.x * 8, 0, SC1);
    }
    if (w != 0) {
#pragma unroll 1
      for (unsigned n = 0; n < (SPIN_MAX << 6); ++n) { if (__builtin_amdgcn_readfirstlane(*(volatile LAS unsigned*)lds<unsigned>(L_CNT))) break; __builtin_amdgcn_s_sleep(1); }
    }
    ST(3);
    {
      const unsigned off = WS_QG + ((g * 4 + b) * 4 * 512 + w * 128 + lane * 2) * 8;
      v4u qv[4];
      _Pragma("unroll 1") for (unsigned n = 0; n < SPIN_MAX; ++n) {
 CBAR();
#pragma unroll
        for (int i = 0; i < 4; i++) qv[i] = __builtin_amdgcn_raw_buffer_load_b128(rws, off, i * 4096, SC1);
        unsigned bad = 0;
#pragma unroll
        for (int i = 0; i < 4; i++) bad |= (qv[i].y ^ e_lo) | (qv[i].w ^ e_lo) | (((qv[i].x ^ tag8) | (qv[i].z ^ tag8)) & 0xFFu);
        if (!__any(bad != 0)) break;
      }
      float q0 = 0.f, q1 = 0.f;
#pragma unroll
      for (int i = 0; i < 4; i++) { q0 += __uint_as_float(qv[i].x & 0xFFFFFF00u); q1 += __uint_as_float(qv[i].z & 0xFFFFFF00u); }
      *lds<unsigned>(L_Q + w * 256 + lane * 4) = pk(q0, q1);
      set_flag(L_FQ, w);
    }
    ST(4);
  }

  const int t = w;
  __builtin_amdgcn_s_setprio(0);
  const rsrc_t rwo = mk(wo, 4096 * 896), rwd = mk(wd, 1792 * 896);
  wait_flags(L_FVI);
  ST(5);
  {
    const unsigned idx = (r * 8 + t) * 14 + lane;
    unsigned pf0 = 0, pf1 = 0, pf2 = 0;
    if (lane < 14) {
      pf0 = __builtin_amdgcn_raw_buffer_load_b32(rwo, g * 512 * 896 + idx * 128, 0, NT_W);
      const unsigned i2 = idx >= 1792 ? idx - 1792 : idx;
      pf1 = __builtin_amdgcn_raw_buffer_load_b32(mk(r >= 16 ? wu : wg, 448 * 3584), (i2 >> 2) * 3584 + ((448 * g) & ~127) + (i2 & 3) * 128, 0, NT_W);
    }
    if (lane < 7 && r < 28) pf2 = __builtin_amdgcn_raw_buffer_load_b32(rwd, g * 224 * 896 + ((r * 8 + t) * 7 + lane) * 128, 0, NT_W);
    if (r >= 21) { asm volatile("" ::"v"(pf0), "v"(pf1), "v"(pf2)); ST_FLUSH(); return; }
    _Pragma("unroll 1") for (unsigned n = 0; n < SPIN_MAX; ++n) {
 CBAR();
      const v4u f0 = __builtin_amdgcn_raw_buffer_load_b128(rws, WS_FLAG + lane * 32, 0, SC1), f1 = __builtin_amdgcn_raw_buffer_load_b128(rws, WS_FLAG + lane * 32 + 16, 0, SC1);
      const unsigned bad = (f0.x ^ e_lo) | (f0.z ^ e_lo) | (f1.x ^ e_lo) | (f1.z ^ e_lo) | (f0.y ^ e_hi) | (f0.w ^ e_hi) | (f1.y ^ e_hi) | (f1.w ^ e_hi);
      if (!__any(bad != 0)) break;
    }
    asm volatile("" ::"v"(pf0), "v"(pf1), "v"(pf2));
  }
  ST(6);
  const int c16 = lane & 15, gg = lane >> 4, bl = (lane >> 2) & 3, kk = lane & 3;
  if (r < 14) {
    unsigned wv[16];
    const unsigned vo = (g * 512 + t * 64 + gg * 16) * 896 + r * 64 + c16 * 4;
#pragma unroll
    for (int n = 0; n < 16; n++) wv[n] = __builtin_amdgcn_raw_buffer_load_b32(rwo, vo, n * 896, NT_W);
    float ws_[8];
    {
      v2u st[8];
      const unsigned so = WS_ST + g * 512 + bl * 128 + (t >> 2) * 8;
      _Pragma("unroll 1") for (unsigned n = 0; n < SPIN_MAX; ++n) {
 CBAR();
        unsigned mx = 0;
#pragma unroll
        for (int i = 0; i < 8; i++) { st[i] = __builtin_amdgcn_raw_buffer_load_b64(rws, so, i * 16, SC1); mx = max(mx, max(st[i].x, st[i].y)); }
        if (!__any(mx == SENT)) break;
      }
      float M = __uint_as_float(st[0].x);
#pragma unroll
      for (int i = 1; i < 8; i++) M = fmaxf(M, __uint_as_float(st[i].x));
      float L = 0.f;
#pragma unroll
      for (int i = 0; i < 8; i++) { ws_[i] = __expf(__uint_as_float(st[i].x) - M); L = fmaf(ws_[i], __uint_as_float(st[i].y), L); }
      const float rl = 1.f / L;
#pragma unroll
      for (int i = 0; i < 8; i++) ws_[i] *= rl;
    }
    v4u ov[8];
    const unsigned oo = WS_OP + (g * 8 + t) * 8192 + bl * 256 + gg * 64 + kk * 16;
    v4u bw[2][2];
#pragma unroll
    for (int e = 0; e < 2; e++)
#pragma unroll
      for (int hh = 0; hh < 2; hh++) bw[e][hh] = v4u{prm(wv[8 * hh + 1], wv[8 * hh], e), prm(wv[8 * hh + 3], wv[8 * hh + 2], e), prm(wv[8 * hh + 5], wv[8 * hh + 4], e), prm(wv[8 * hh + 7], wv[8 * hh + 6], e)};
    PIN4(bw[0][0], bw[0][1], bw[1][0], bw[1][1]);
    PIN4(ws_[0], ws_[1], ws_[2], ws_[3]); PIN4(ws_[4], ws_[5], ws_[6], ws_[7]);
    SB();
    ST(7);
    poll_issue<8, 1024>(sws, oo, ov);
    poll_finish<8, 1024>(sws, oo, ov);
    ST(8);
    v4f o = {0.f, 0.f, 0.f, 0.f};
#pragma unroll
    for (int i = 0; i < 8; i++) o += ws_[i] * __builtin_bit_cast(v4f, ov[i]);
    const unsigned x0 = pk(o[0], o[1]), x1 = pk(o[2], o[3]);
    const v4u a0 = {dppu<0x00>(x0), dppu<0x00>(x1), dppu<0x55>(x0), dppu<0x55>(x1)}, a1 = {dppu<0xAA>(x0), dppu<0xAA>(x1), dppu<0xFF>(x0), dppu<0xFF>(x1)};
    const v4f z = {0.f, 0.f, 0.f, 0.f};
    const v4f de = mfma(a1, bw[0][1], mfma(a0, bw[0][0], z)), dd = mfma(a1, bw[1][1], mfma(a0, bw[1][0], z));
    *lds<v2f>(L_RED + t * 512 + lane * 8) = v2f{de[0], dd[0]};
    wg_barrier();
    if (t == 0) {
      v2f a = *lds<v2f>(L_RED + lane * 8);
#pragma unroll
      for (int i = 1; i < 8; i++) a += *lds<v2f>(L_RED + i * 512 + lane * 8);
      __builtin_amdgcn_raw_buffer_store_b64(__builtin_bit_cast(v2u, a), rws, WS_HP + (g * 14 + r) * 512 + lane * 8, 0, SC1);
    }
    ST(9);
    ST_FLUSH();
    return;
  }
  const int f = g * 7 + r - 14;
  if (t < 7) {
    unsigned gv[16], uv[16];
    v2u dv[8];
    const rsrc_t rg = mk(wg, 448 * 3584), ru = mk(wu, 448 * 3584);
    const unsigned vo = (t * 64 + gg * 16) * 3584 + f * 64 + c16 * 4;
    const v2u xr = __builtin_amdgcn_raw_buffer_load_b64(mk(x, 3584), bl * 896 + t * 128 + gg * 32 + kk * 8, 0, 0);
#pragma unroll
    for (int n = 0; n < 16; n++) gv[n] = __builtin_amdgcn_raw_buffer_load_b32(rg, vo, n * 3584, NT_W);
#pragma unroll
    for (int n = 0; n < 16; n++) uv[n] = __builtin_amdgcn_raw_buffer_load_b32(ru, vo, n * 3584, NT_W);
    const unsigned dvo = (f * 32 + gg * 8) * 896 + t * 128 + c16 * 8;
#pragma unroll
    for (int n = 0; n < 8; n++) dv[n] = __builtin_amdgcn_raw_buffer_load_b64(rwd, dvo, n * 896, NT_W);
    v4u bg[2][2], bu[2][2], bd[4];
#pragma unroll
    for (int e = 0; e < 2; e++)
#pragma unroll
      for (int hh = 0; hh < 2; hh++) {
        bg[e][hh] = v4u{prm(gv[8 * hh + 1], gv[8 * hh], e), prm(gv[8 * hh + 3], gv[8 * hh + 2], e), prm(gv[8 * hh + 5], gv[8 * hh + 4], e), prm(gv[8 * hh + 7], gv[8 * hh + 6], e)};
        bu[e][hh] = v4u{prm(uv[8 * hh + 1], uv[8 * hh], e), prm(uv[8 * hh + 3], uv[8 * hh + 2], e), prm(uv[8 * hh + 5], uv[8 * hh + 4], e), prm(uv[8 * hh + 7], uv[8 * hh + 6], e)};
      }
#pragma unroll
    for (int i = 0; i < 4; i++) bd[i] = v4u{prm(dv[1][i >> 1], dv[0][i >> 1], i & 1), prm(dv[3][i >> 1], dv[2][i >> 1], i & 1), prm(dv[5][i >> 1], dv[4][i >> 1], i & 1), prm(dv[7][i >> 1], dv[6][i >> 1], i & 1)};
    PIN4(bg[0][0], bg[0][1], bg[1][0], bg[1][1]); PIN4(bu[0][0], bu[0][1], bu[1][0], bu[1][1]); PIN4(bd[0], bd[1], bd[2], bd[3]);
    float xr0 = lo_f(xr.x), xr1 = hi_f(xr.x), xr2 = lo_f(xr.y), xr3 = hi_f(xr.y);
    PIN4(xr0, xr1, xr2, xr3);
    SB();
    ST(7);
    v4u hv[8];
    const unsigned ho = WS_HP + (2 * t + (gg >> 1)) * 512 + bl * 128 + (gg & 1) * 64 + kk * 16;
    poll_issue<8, 7168>(sws, ho, hv);
    poll_finish<8, 7168>(sws, ho, hv);
    ST(8);
    v4f a = __builtin_bit_cast(v4f, hv[0]);
#pragma unroll
    for (int i = 1; i < 8; i++) a += __builtin_bit_cast(v4f, hv[i]);
    const unsigned a01 = pk(a[0], a[1]), a23 = pk(a[2], a[3]);
    const unsigned h01 = pk(xr0 + lo_f(a01), xr1 + hi_f(a01)), h23 = pk(xr2 + lo_f(a23), xr3 + hi_f(a23));
    *lds<v2u>(L_HB + bl * 896 + t * 128 + gg * 32 + kk * 8) = v2u{h01, h23};
    const float h0 = lo_f(h01), h1 = hi_f(h01), h2 = lo_f(h23), h3 = hi_f(h23);
    float sm = (h0 + h1) + (h2 + h3);
    sm += dppf<XOR1>(sm); sm += dppf<XOR2>(sm); sm = x16_sum(sm);
    const float mw = sm * (1.f / 64.f);
    float m2 = (h0 - mw) * (h0 - mw);
    m2 = fmaf(h1 - mw, h1 - mw, m2); m2 = fmaf(h2 - mw, h2 - mw, m2); m2 = fmaf(h3 - mw, h3 - mw, m2);
    m2 += dppf<XOR1>(m2); m2 += dppf<XOR2>(m2); m2 = x16_sum(m2);
    if (lane < 16 && kk == 0) *lds<v2f>(L_STAT + bl * 64 + t * 8) = v2f{mw, m2};
    wg_barrier();
    float mean = 0.f, M2 = 0.f;
    v2f sw[7];
#pragma unroll
    for (int i = 0; i < 7; i++) { sw[i] = *lds<v2f>(L_STAT + bl * 64 + i * 8); mean += sw[i][0]; M2 += sw[i][1]; }
    mean *= (1.f / 7.f);
    float dsum = 0.f;
#pragma unroll
    for (int i = 0; i < 7; i++) dsum = fmaf(sw[i][0] - mean, sw[i][0] - mean, dsum);
    const float var = fmaf(dsum, 64.f, M2) * (1.f / 448.f);
    const float mb = rbf(mean), den = rbf(__builtin_amdgcn_sqrtf(rbf(rbf(var) + 1e-5f))), ri = __builtin_amdgcn_rcpf(den);
    const unsigned d01 = pk(h0 - mb, h1 - mb), d23 = pk(h2 - mb, h3 - mb);
    const unsigned x0 = pk(lo_f(d01) * ri, hi_f(d01) * ri), x1 = pk(lo_f(d23) * ri, hi_f(d23) * ri);
    const v4u a0 = {dppu<0x00>(x0), dppu<0x00>(x1), dppu<0x55>(x0), dppu<0x55>(x1)}, a1 = {dppu<0xAA>(x0), dppu<0xAA>(x1), dppu<0xFF>(x0), dppu<0xFF>(x1)};
    const v4f z = {0.f, 0.f, 0.f, 0.f};
    const v4f ge = mfma(a1, bg[0][1], mfma(a0, bg[0][0], z)), go = mfma(a1, bg[1][1], mfma(a0, bg[1][0], z));
    const v4f ue = mfma(a1, bu[0][1], mfma(a0, bu[0][0], z)), uo = mfma(a1, bu[1][1], mfma(a0, bu[1][0], z));
    *lds<v4f>(L_RED + t * 1024 + lane * 16) = v4f{ge[0], go[0], ue[0], uo[0]};
    wg_barrier();
    ST(9);
    v4f gu = *lds<v4f>(L_RED + (bl * 16 + gg * 4 + kk) * 16);
#pragma unroll
    for (int i = 1; i < 7; i++) gu += *lds<v4f>(L_RED + i * 1024 + (bl * 16 + gg * 4 + kk) * 16);
    const unsigned gp = pk(gu[0], gu[1]), up = pk(gu[2], gu[3]);
    const float g0 = lo_f(gp), g1 = hi_f(gp);
    const unsigned sp = pk(g0 * __builtin_amdgcn_rcpf(1.f + __expf(-g0)), g1 * __builtin_amdgcn_rcpf(1.f + __expf(-g1)));
    const unsigned xa = pk(lo_f(sp) * lo_f(up), hi_f(sp) * hi_f(up));
    const v4u aa = {dppu<0x00>(xa), dppu<0x55>(xa), dppu<0xAA>(xa), dppu<0xFF>(xa)};
    v4f o;
#pragma unroll
    for (int i = 0; i < 4; i++) o[i] = mfma(aa, bd[i], z)[0];
    __builtin_amdgcn_raw_buffer_store_b128(__builtin_bit_cast(v4u, o), rws, WS_FP + f * 7168 + gg * 1792 + t * 256 + c16 * 16, 0, SC1);
    ST(10);
  } else {
    const int ob = f / 14, oc = f % 14;
    wg_barrier();
    const v2u hr = *lds<v2u>(L_HB + ob * 896 + oc * 64 + (lane & 7) * 8);
    wg_barrier();
    ST(9);
    v4u fv[7];
    const unsigned fo = WS_FP + (lane >> 3) * 7168 + ob * 1792 + oc * 128 + (lane & 7) * 16;
    poll_issue<7, 8 * 7168>(sws, fo, fv);
    poll_finish<7, 8 * 7168>(sws, fo, fv);
    ST(10);
    v4f a = __builtin_bit_cast(v4f, fv[0]);
#pragma unroll
    for (int i = 1; i < 7; i++) a += __builtin_bit_cast(v4f, fv[i]);
#pragma unroll
    for (int i = 0; i < 4; i++) { a[i] += dppf<ROR8>(a[i]); a[i] = x16_sum(a[i]); }
    const unsigned a01 = pk(a[0], a[1]), a23 = pk(a[2], a[3]);
    const v2u o = {pk(lo_f(hr.x) + lo_f(a01), hi_f(hr.x) + hi_f(a01)), pk(lo_f(hr.y) + lo_f(a23), hi_f(hr.y) + hi_f(a23))};
    if (lane < 8) __builtin_amdgcn_raw_buffer_store_b64(o, mk(out, 3584), ob * 896 + oc * 64 + lane * 8, 0, 0);
    if (f == 0 && lane == 0) __builtin_amdgcn_raw_buffer_store_b64(v2u{e_lo, e_hi}, rws, WS_EPOCH, 0, SC1);
    ST(11);
  }
  ST_FLUSH();
}
}

extern "C" void kernel_launch(void* const* d_in, const int* in_sizes, int n_in, void* d_out, int out_size, void* d_ws, size_t ws_size, hipStream_t stream) {
  (void)in_sizes; (void)n_in; (void)out_size; (void)ws_size;
  fk::fused_block<<<256, 768, 0, stream>>>((const char*)d_in[0], (const char*)d_in[1], (const char*)d_in[2], (const char*)d_in[3], (const char*)d_in[4],
                                                       (const char*)d_in[6], (const char*)d_in[7], (const char*)d_in[8], (char*)d_out, (char*)d_ws);
}
```

```cpp
#include <hip/hip_runtime.h>

__shared__ __attribute__((aligned(16))) char fk_smem[16384];

__global__ void __launch_bounds__(768) fused_block(const void* x, const void* kc, const void* vc, const void* wq, const void* wo,
                                                   const void* wg, const void* wu, const void* wd, void* out, void* ws) {
  asm volatile(
    ".Lfk_top:\n"
    "s_load_dwordx16 s[4:19], s[0:1], 0\n"
    "s_load_dwordx4 s[20:23], s[0:1], 64\n"
    "s_bfe_u32 s28, s2, 0x30003\n"
    "v_readfirstlane_b32 s24, v0\n"
    "v_and_b32 v1, 63, v0\n"
    "s_mov_b32 s39, 0x20000\n"
    "v_mov_b32 v0, 0\n"
    "s_lshr_b32 s24, s24, 6\n"
    "s_and_b32 s30, s24, 3\n"
    "s_cmp_lt_u32 s24, 8\n"
    "s_cbranch_scc1 .Lfk_pre1\n"
    "s_mul_i32 s54, s30, 896\n"
    "v_lshlrev_b32 v2, 4, v1\n"
    "s_movk_i32 s38, 896\n"
    "s_waitcnt lgkmcnt(0)\n"
    "s_add_u32 s36, s4, s54\n"
    "s_addc_u32 s37, s5, 0\n"
    "buffer_load_dwordx4 v[4:7], v2, s[36:39], 0 offen\n"
    "s_barrier\n"
    "s_memrealtime s[60:61]\n"
    "s_branch .Lfk_V2\n"
    ".Lfk_pre1:\n"
    "s_and_b32 s25, s2, 7\n"
    "s_lshr_b32 s27, s2, 6\n"
    "s_cmp_ge_u32 s24, 4\n"
    "s_cbranch_scc1 .Lfk_K\n"
    "s_cmp_eq_u32 s24, 0\n"
    "s_cbranch_scc1 .Lfk_pre0\n"
    "s_cmp_lg_u32 s24, 1\n"
    "s_cbranch_scc1 .Lfk_pre23\n"
    "v_lshlrev_b32 v2, 2, v1\n"
    "ds_write_b32 v2, v0\n"
    "s_waitcnt lgkmcnt(0)\n"
    "global_load_dword v4, v0, s[22:23] sc1\n"
    "s_barrier\n"
    "s_branch .Lfk_EG\n"
    ".Lfk_pre23:\n"
    "s_barrier\n"
    "s_waitcnt lgkmcnt(0)\n"
    "s_branch .Lfk_EG\n"
    ".Lfk_pre0:\n"
    "v_lshrrev_b32 v2, 3, v1\n"
    "v_and_b32 v3, 7, v1\n"
    "v_mul_u32_u24 v2, 0x1c000, v2\n"
    "s_mul_i32 s53, s27, 7168\n"
    "s_xor_b32 s3, s25, 0\n"
    "s_lshl3_add_u32 s54, s3, s28\n"
    "v_lshl_add_u32 v2, v3, 4, v2\n"
    "s_add_u32 s53, s53, s54\n"
    "s_lshl_b32 s53, s53, 7\n"
    "s_mov_b32 s38, 909440\n"
    "s_movk_i32 s54, 0x2000\n"
    "s_mov_b32 s55, 0\n"
    "s_waitcnt lgkmcnt(0)\n"
    "s_add_u32 s36, s10, s53\n"
    "s_addc_u32 s37, s11, 0\n"
    "buffer_load_dwordx4 v[8:11], v2, s[36:39], s55 offen nt\n"
    "s_add_u32 s55, s55, s54\n"
    "buffer_load_dwordx4 v[12:15], v2, s[36:39], s55 offen nt\n"
    "s_add_u32 s55, s55, s54\n"
    "buffer_load_dwordx4 v[16:19], v2, s[36:39], s55 offen nt\n"
    "s_add_u32 s55, s55, s54\n"
    "buffer_load_dwordx4 v[20:23], v2, s[36:39], s55 offen nt\n"
    "s_add_u32 s55, s55, s54\n"
    "buffer_load_dwordx4 v[24:27], v2, s[36:39], s55 offen nt\n"
    "s_add_u32 s55, s55, s54\n"
    "buffer_load_dwordx4 v[28:31], v2, s[36:39], s55 offen nt\n"
    "s_add_u32 s55, s55, s54\n"
    "buffer_load_dwordx4 v[32:35], v2, s[36:39], s55 offen nt\n"
    "s_add_u32 s55, s55, s54\n"
    "buffer_load_dwordx4 v[36:39], v2, s[36:39], s55 offen nt\n"
    "s_add_u32 s55, s55, s54\n"
    "buffer_load_dwordx4 v[40:43], v2, s[36:39], s55 offen nt\n"
    "s_add_u32 s55, s55, s54\n"
    "buffer_load_dwordx4 v[44:47], v2, s[36:39], s55 offen nt\n"
    "s_add_u32 s55, s55, s54\n"
    "buffer_load_dwordx4 v[48:51], v2, s[36:39], s55 offen nt\n"
    "s_add_u32 s55, s55, s54\n"
    "buffer_load_dwordx4 v[52:55], v2, s[36:39], s55 offen nt\n"
    "s_add_u32 s55, s55, s54\n"
    "buffer_load_dwordx4 v[56:59], v2, s[36:39], s55 offen nt\n"
    "s_add_u32 s55, s55, s54\n"
    "buffer_load_dwordx4 v[60:63], v2, s[36:39], s55 offen nt\n"
    "s_barrier\n"
    "s_branch .Lfk_Q\n"
    ".Lfk_K:\n"
    "s_lshl3_add_u32 s53, s27, s25\n"
    "s_lshl1_add_u32 s29, s25, s28\n"
    "s_lshl_b32 s53, s53, 5\n"
    "s_lshl3_add_u32 s29, s27, s29\n"
    "s_add_u32 s53, s53, s28\n"
    "s_add_u32 s53, s53, 24\n"
    "v_bfe_u32 v2, v1, 1, 3\n"
    "v_lshrrev_b32 v4, 4, v1\n"
    "s_lshl_b32 s53, s53, 16\n"
    "v_lshlrev_b32 v2, 9, v2\n"
    "v_and_b32 v5, 1, v1\n"
    "v_lshl_add_u32 v2, v4, 5, v2\n"
    "s_add_u32 s55, s29, s30\n"
    "v_lshl_add_u32 v2, v5, 4, v2\n"
    "s_lshl_b32 s55, s55, 12\n"
    "s_mov_b32 s38, 0x10000\n"
    "s_and_b32 s55, s55, 0xffff\n"
    "s_waitcnt lgkmcnt(0)\n"
    "s_add_u32 s36, s6, s53\n"
    "s_addc_u32 s37, s7, 0\n"
    "s_barrier\n"
    "buffer_load_dwordx4 v[8:11], v2, s[36:39], s55 offen offset:0 nt\n"
    "buffer_load_dwordx4 v[12:15], v2, s[36:39], s55 offen offset:128 nt\n"
    "buffer_load_dwordx4 v[16:19], v2, s[36:39], s55 offen offset:256 nt\n"
    "buffer_load_dwordx4 v[20:23], v2, s[36:39], s55 offen offset:384 nt\n"
    "s_add_u32 s55, s55, 0x4000\n"
    "s_and_b32 s55, s55, 0xffff\n"
    "buffer_load_dwordx4 v[24:27], v2, s[36:39], s55 offen offset:0 nt\n"
    "buffer_load_dwordx4 v[28:31], v2, s[36:39], s55 offen offset:128 nt\n"
    "s_lshr_b32 s26, s2, 3\n"
    "s_lshl2_add_u32 s56, s26, s30\n"
    "s_mul_i32 s56, s56, 28\n"
    "v_add_u32 v3, s56, v1\n"
    "v_lshlrev_b32 v3, 7, v3\n"
    "s_xor_b32 s3, s25, 4\n"
    "s_mul_i32 s56, s3, 458752\n"
    "s_add_u32 s44, s12, s56\n"
    "s_addc_u32 s45, s13, 0\n"
    "s_and_b32 s45, s45, 0xffff\n"
    "s_mov_b32 s46, 458752\n"
    "s_mov_b32 s47, 0x20000\n"
    "s_mov_b32 s32, s22\n"
    "s_and_b32 s33, s23, 0xffff\n"
    "s_mov_b32 s34, 1712128\n"
    "s_mov_b32 s35, 0x20000\n"
    "s_lshl_b32 s59, s30, 2\n"
    "v_mov_b32 v104, s59\n"
    "v_bfe_u32 v7, v1, 1, 1\n"
    "v_lshlrev_b32 v105, 9, v7\n"
    "v_lshl_add_u32 v105, v4, 5, v105\n"
    "v_lshl_add_u32 v105, v5, 4, v105\n"
    "v_lshlrev_b32 v7, 4, v7\n"
    "v_add_u32 v6, s59, v7\n"
    "v_lshlrev_b32 v106, 6, v4\n"
    "v_lshl_add_u32 v106, v5, 5, v106\n"
    "v_lshl_add_u32 v106, v7, 4, v106\n"
    "v_and_b32 v4, 12, v1\n"
    "s_lshl_b32 s59, s30, 1\n"
    "v_lshl_add_u32 v106, v4, 1, v106\n"
    "v_add_u32 v106, s59, v106\n"
    "s_mov_b32 s56, 0xaaaaaaaa\n"
    "s_mov_b32 s57, 0xaaaaaaaa\n"
    "s_waitcnt vmcnt(5)\n"
    "buffer_load_dwordx4 v[32:35], v2, s[36:39], s55 offen offset:256 nt\n"
    "s_waitcnt vmcnt(5)\n"
    "buffer_load_dwordx4 v[36:39], v2, s[36:39], s55 offen offset:384 nt\n"
    "s_add_u32 s55, s55, 0x4000\n"
    "s_and_b32 s55, s55, 0xffff\n"
    "s_waitcnt vmcnt(5)\n"
    "buffer_load_dwordx4 v[40:43], v2, s[36:39], s55 offen offset:0 nt\n"
    "s_waitcnt vmcnt(5)\n"
    "buffer_load_dwordx4 v[44:47], v2, s[36:39], s55 offen offset:128 nt\n"
    "s_waitcnt vmcnt(5)\n"
    "buffer_load_dwordx4 v[48:51], v2, s[36:39], s55 offen offset:256 nt\n"
    "s_waitcnt vmcnt(5)\n"
    "buffer_load_dwordx4 v[52:55], v2, s[36:39], s55 offen offset:384 nt\n"
    "s_add_u32 s55, s55, 0x4000\n"
    "s_and_b32 s55, s55, 0xffff\n"
    "s_waitcnt vmcnt(5)\n"
    "buffer_load_dwordx4 v[56:59], v2, s[36:39], s55 offen offset:0 nt\n"
    "s_waitcnt vmcnt(5)\n"
    "buffer_load_dwordx4 v[60:63], v2, s[36:39], s55 offen offset:128 nt\n"
    "s_waitcnt vmcnt(5)\n"
    "buffer_load_dwordx4 v[64:67], v2, s[36:39], s55 offen offset:256 nt\n"
    "s_waitcnt vmcnt(5)\n"
    "buffer_load_dwordx4 v[68:71], v2, s[36:39], s55 offen offset:384 nt\n"
    "v_mov_b32 v5, 1\n"
    "ds_write_b32 v104, v5 offset:48\n"
    "s_waitcnt vmcnt(5)\n"
    "v_mov_b32 v5, 2\n"
    "ds_write_b32 v104, v5 offset:48\n"
    "s_waitcnt vmcnt(4)\n"
    "v_mov_b32 v5, 3\n"
    "ds_write_b32 v104, v5 offset:48\n"
    "s_waitcnt vmcnt(3)\n"
    "v_mov_b32 v5, 4\n"
    "ds_write_b32 v104, v5 offset:48\n"
    "s_waitcnt vmcnt(2)\n"
    "v_mov_b32 v5, 5\n"
    "ds_write_b32 v104, v5 offset:48\n"
    "s_waitcnt vmcnt(1)\n"
    "v_mov_b32 v5, 6\n"
    "ds_write_b32 v104, v5 offset:48\n"
    "s_waitcnt vmcnt(0)\n"
    "s_mov_b32 s63, 0x400000\n"
    ".Lfk_sp1:\n"
    "ds_read_b32 v5, v0 offset:12\n"
    "s_sub_u32 s63, s63, 1\n"
    "s_cbranch_scc1 .Lfk_so2\n"
    "s_waitcnt lgkmcnt(0)\n"
    "v_cmp_gt_u32 vcc, 2, v5\n"
    "s_cbranch_vccz .Lfk_so2\n"
    "s_sleep 1\n"
    "s_branch .Lfk_sp1\n"
    ".Lfk_so2:\n"
    "s_waitcnt lgkmcnt(0)\n"
    "ds_read_b128 v[72:75], v105 offset:4096\n"
    "ds_read_b128 v[76:79], v105 offset:4224\n"
    "ds_read_b128 v[80:83], v105 offset:4352\n"
    "ds_read_b128 v[84:87], v105 offset:4480\n"
    "s_waitcnt lgkmcnt(3)\n"
    "v_mfma_f32_16x16x32_bf16 v[88:91], v[8:11], v[72:75], 0\n"
    "v_mfma_f32_16x16x32_bf16 v[92:95], v[24:27], v[72:75], 0\n"
    "v_mfma_f32_16x16x32_bf16 v[96:99], v[40:43], v[72:75], 0\n"
    "v_mfma_f32_16x16x32_bf16 v[100:103], v[56:59], v[72:75], 0\n"
    "s_waitcnt lgkmcnt(2)\n"
    "v_mfma_f32_16x16x32_bf16 v[88:91], v[12:15], v[76:79], v[88:91]\n"
    "v_mfma_f32_16x16x32_bf16 v[92:95], v[28:31], v[76:79], v[92:95]\n"
    "v_mfma_f32_16x16x32_bf16 v[96:99], v[44:47], v[76:79], v[96:99]\n"
    "v_mfma_f32_16x16x32_bf16 v[100:103], v[60:63], v[76:79], v[100:103]\n"
    "s_waitcnt lgkmcnt(1)\n"
    "v_mfma_f32_16x16x32_bf16 v[88:91], v[16:19], v[80:83], v[88:91]\n"
    "v_mfma_f32_16x16x32_bf16 v[92:95], v[32:35], v[80:83], v[92:95]\n"
    "v_mfma_f32_16x16x32_bf16 v[96:99], v[48:51], v[80:83], v[96:99]\n"
    "v_mfma_f32_16x16x32_bf16 v[100:103], v[64:67], v[80:83], v[100:103]\n"
    "s_waitcnt lgkmcnt(0)\n"
    "v_mfma_f32_16x16x32_bf16 v[88:91], v[20:23], v[84:87], v[88:91]\n"
    "v_mfma_f32_16x16x32_bf16 v[92:95], v[36:39], v[84:87], v[92:95]\n"
    "v_mfma_f32_16x16x32_bf16 v[96:99], v[52:55], v[84:87], v[96:99]\n"
    "v_mfma_f32_16x16x32_bf16 v[100:103], v[68:71], v[84:87], v[100:103]\n"
    "s_nop 4\n"
    "v_add_f32_dpp v4, v89, v88 quad_perm:[1,0,3,2] row_mask:0xf bank_mask:0x1\n"
    "v_add_f32_dpp v5, v90, v91 quad_perm:[1,0,3,2] row_mask:0xf bank_mask:0x1\n"
    "s_nop 0\n"
    "v_add_f32_dpp v4, v93, v92 quad_perm:[1,0,3,2] row_mask:0xf bank_mask:0x2\n"
    "v_add_f32_dpp v5, v94, v95 quad_perm:[1,0,3,2] row_mask:0xf bank_mask:0x2\n"
    "s_nop 0\n"
    "v_add_f32_dpp v4, v97, v96 quad_perm:[1,0,3,2] row_mask:0xf bank_mask:0x4\n"
    "v_add_f32_dpp v5, v98, v99 quad_perm:[1,0,3,2] row_mask:0xf bank_mask:0x4\n"
    "s_nop 0\n"
    "v_add_f32_dpp v4, v101, v100 quad_perm:[1,0,3,2] row_mask:0xf bank_mask:0x8\n"
    "v_add_f32_dpp v5, v102, v103 quad_perm:[1,0,3,2] row_mask:0xf bank_mask:0x8\n"
    "v_cndmask_b32 v4, v4, v5, s[56:57]\n"
    "v_cvt_pk_bf16_f32 v4, v4, v4\n"
    "v_lshlrev_b32 v4, 16, v4\n"
    "s_nop 1\n"
    "v_max_f32_dpp v5, v4, v4 quad_perm:[1,0,3,2] row_mask:0xf bank_mask:0xf\n"
    "s_nop 1\n"
    "v_max_f32_dpp v5, v5, v5 row_ror:4 row_mask:0xf bank_mask:0xf\n"
    "s_nop 1\n"
    "v_max_f32_dpp v5, v5, v5 row_ror:8 row_mask:0xf bank_mask:0xf\n"
    "v_mov_b32 v2, v5\n"
    "s_nop 1\n"
    "v_permlane16_swap_b32 v5, v2\n"
    "v_max_f32 v5, v5, v2\n"
    "v_mov_b32 v2, v5\n"
    "s_nop 1\n"
    "v_permlane32_swap_b32 v5, v2\n"
    "v_max_f32 v5, v5, v2\n"
    "ds_write_b32 v6, v5 offset:128\n"
    "s_mov_b64 exec, 1\n"
    "v_mov_b32 v5, 1\n"
    "ds_add_u32 v0, v5 offset:16\n"
    "s_mov_b64 exec, -1\n"
    "s_mov_b32 s63, 0x400000\n"
    ".Lfk_sp3:\n"
    "ds_read_b32 v5, v0 offset:16\n"
    "s_sub_u32 s63, s63, 1\n"
    "s_cbranch_scc1 .Lfk_so4\n"
    "s_waitcnt lgkmcnt(0)\n"
    "v_cmp_gt_u32 vcc, 4, v5\n"
    "s_cbranch_vccnz .Lfk_sp3\n"
    ".Lfk_so4:\n"
    "s_waitcnt lgkmcnt(0)\n"
    "ds_read_b128 v[88:91], v7 offset:128\n"
    "s_waitcnt lgkmcnt(0)\n"
    "v_max3_f32 v5, v88, v89, v90\n"
    "v_max_f32 v5, v5, v91\n"
    "v_sub_f32 v4, v4, v5\n"
    "v_mul_f32 v4, 0x3db8aa3b, v4\n"
    "v_exp_f32 v4, v4\n"
    "s_nop 0\n"
    "v_cvt_pk_bf16_f32 v5, v4, v4\n"
    "ds_write_b16 v106, v5 offset:5120\n"
    "v_add_f32_dpp v4, v4, v4 quad_perm:[1,0,3,2] row_mask:0xf bank_mask:0xf\n"
    "s_nop 1\n"
    "v_add_f32_dpp v4, v4, v4 row_ror:4 row_mask:0xf bank_mask:0xf\n"
    "s_nop 1\n"
    "v_add_f32_dpp v4, v4, v4 row_ror:8 row_mask:0xf bank_mask:0xf\n"
    "v_mov_b32 v5, v4\n"
    "s_nop 1\n"
    "v_permlane16_swap_b32 v4, v5\n"
    "v_add_f32 v4, v4, v5\n"
    "v_mov_b32 v5, v4\n"
    "s_nop 1\n"
    "v_permlane32_swap_b32 v4, v5\n"
    "v_add_f32 v4, v4, v5\n"
    "ds_write_b32 v6, v4 offset:160\n"
    "s_mov_b64 exec, 1\n"
    "v_mov_b32 v5, 1\n"
    "ds_add_u32 v0, v5 offset:20\n"
    "s_mov_b64 exec, -1\n"
    "s_waitcnt vmcnt(0)\n"
    "s_branch .Lfk_tail\n"
    ".Lfk_V2:\n"
    "s_setprio 3\n"
    "s_and_b32 s25, s2, 7\n"
    "s_lshr_b32 s27, s2, 6\n"
    "s_lshr_b32 s26, s2, 3\n"
    "s_lshl3_add_u32 s53, s27, s25\n"
    "s_lshl1_add_u32 s29, s25, s28\n"
    "s_lshl_b32 s53, s53, 5\n"
    "s_lshl3_add_u32 s29, s27, s29\n"
    "s_add_u32 s53, s53, s28\n"
    "s_add_u32 s53, s53, 24\n"
    "s_waitcnt vmcnt(0)\n"
    "v_lshlrev_b32 v8, 16, v4\n"
    "v_and_b32 v9, 0xffff0000, v4\n"
    "v_lshlrev_b32 v10, 16, v5\n"
    "v_and_b32 v11, 0xffff0000, v5\n"
    "v_lshlrev_b32 v12, 16, v6\n"
    "v_and_b32 v13, 0xffff0000, v6\n"
    "v_lshlrev_b32 v14, 16, v7\n"
    "v_and_b32 v15, 0xffff0000, v7\n"
    "v_pk_add_f32 v[16:17], v[8:9], v[10:11]\n"
    "v_pk_add_f32 v[18:19], v[12:13], v[14:15]\n"
    "v_pk_add_f32 v[16:17], v[16:17], v[18:19]\n"
    "v_add_f32 v16, v16, v17\n"
    "s_nop 1\n"
    "v_add_f32_dpp v16, v16, v16 quad_perm:[1,0,3,2] row_mask:0xf bank_mask:0xf\n"
    "s_nop 1\n"
    "v_add_f32_dpp v16, v16, v16 quad_perm:[2,3,0,1] row_mask:0xf bank_mask:0xf\n"
    "s_nop 1\n"
    "v_add_f32_dpp v16, v16, v16 row_ror:4 row_mask:0xf bank_mask:0xf\n"
    "s_nop 1\n"
    "v_add_f32_dpp v16, v16, v16 row_ror:8 row_mask:0xf bank_mask:0xf\n"
    "s_nop 1\n"
    "v_add_f32_dpp v16, v16, v16 row_bcast:15 row_mask:0xa bank_mask:0xf\n"
    "s_nop 1\n"
    "v_add_f32_dpp v16, v16, v16 row_bcast:31 row_mask:0xc bank_mask:0xf\n"
    "s_nop 0\n"
    "v_readlane_b32 s56, v16, 63\n"
    "v_mov_b32 v20, 0x3b124925\n"
    "v_cmp_gt_u32 vcc, 56, v1\n"
    "v_mul_f32 v18, s56, v20\n"
    "v_pk_add_f32 v[24:25], v[8:9], v[18:19] op_sel_hi:[1,0] neg_lo:[0,1] neg_hi:[0,1]\n"
    "v_pk_add_f32 v[26:27], v[10:11], v[18:19] op_sel_hi:[1,0] neg_lo:[0,1] neg_hi:[0,1]\n"
    "v_pk_add_f32 v[28:29], v[12:13], v[18:19] op_sel_hi:[1,0] neg_lo:[0,1] neg_hi:[0,1]\n"
    "v_pk_add_f32 v[30:31], v[14:15], v[18:19] op_sel_hi:[1,0] neg_lo:[0,1] neg_hi:[0,1]\n"
    "v_pk_mul_f32 v[16:17], v[24:25], v[24:25]\n"
    "v_pk_fma_f32 v[16:17], v[26:27], v[26:27], v[16:17]\n"
    "v_pk_fma_f32 v[16:17], v[28:29], v[28:29], v[16:17]\n"
    "v_pk_fma_f32 v[16:17], v[30:31], v[30:31], v[16:17]\n"
    "v_add_f32 v16, v16, v17\n"
    "v_cndmask_b32 v16, 0, v16, vcc\n"
    "v_cvt_pk_bf16_f32 v18, v18, v18\n"
    "v_lshlrev_b32 v18, 16, v18\n"
    "v_pk_add_f32 v[24:25], v[8:9], v[18:19] op_sel_hi:[1,0] neg_lo:[0,1] neg_hi:[0,1]\n"
    "v_pk_add_f32 v[26:27], v[10:11], v[18:19] op_sel_hi:[1,0] neg_lo:[0,1] neg_hi:[0,1]\n"
    "v_pk_add_f32 v[28:29], v[12:13], v[18:19] op_sel_hi:[1,0] neg_lo:[0,1] neg_hi:[0,1]\n"
    "v_pk_add_f32 v[30:31], v[14:15], v[18:19] op_sel_hi:[1,0] neg_lo:[0,1] neg_hi:[0,1]\n"
    "v_add_f32_dpp v16, v16, v16 quad_perm:[1,0,3,2] row_mask:0xf bank_mask:0xf\n"
    "s_nop 1\n"
    "v_add_f32_dpp v16, v16, v16 quad_perm:[2,3,0,1] row_mask:0xf bank_mask:0xf\n"
    "s_nop 1\n"
    "v_add_f32_dpp v16, v16, v16 row_ror:4 row_mask:0xf bank_mask:0xf\n"
    "s_nop 1\n"
    "v_add_f32_dpp v16, v16, v16 row_ror:8 row_mask:0xf bank_mask:0xf\n"
    "s_nop 1\n"
    "v_add_f32_dpp v16, v16, v16 row_bcast:15 row_mask:0xa bank_mask:0xf\n"
    "s_nop 1\n"
    "v_add_f32_dpp v16, v16, v16 row_bcast:31 row_mask:0xc bank_mask:0xf\n"
    "s_nop 0\n"
    "v_readlane_b32 s55, v16, 63\n"
    "v_cvt_pk_bf16_f32 v4, v24, v25\n"
    "v_cvt_pk_bf16_f32 v5, v26, v27\n"
    "v_cvt_pk_bf16_f32 v6, v28, v29\n"
    "v_cvt_pk_bf16_f32 v7, v30, v31\n"
    "v_mul_f32 v16, s55, v20\n"
    "v_cvt_pk_bf16_f32 v16, v16, v16\n"
    "v_lshlrev_b32 v16, 16, v16\n"
    "v_add_f32 v16, 0x3727c5ac, v16\n"
    "v_cvt_pk_bf16_f32 v16, v16, v16\n"
    "v_lshlrev_b32 v16, 16, v16\n"
    "v_sqrt_f32 v16, v16\n"
    "v_lshlrev_b32 v8, 16, v4\n"
    "v_and_b32 v9, 0xffff0000, v4\n"
    "v_lshlrev_b32 v10, 16, v5\n"
    "v_and_b32 v11, 0xffff0000, v5\n"
    "v_lshlrev_b32 v12, 16, v6\n"
    "v_and_b32 v13, 0xffff0000, v6\n"
    "v_lshlrev_b32 v14, 16, v7\n"
    "v_and_b32 v15, 0xffff0000, v7\n"
    "v_cvt_pk_bf16_f32 v16, v16, v16\n"
    "v_lshlrev_b32 v16, 16, v16\n"
    "v_rcp_f32 v16, v16\n"
    "v_add_u32 v2, s54, v2\n"
    "v_pk_mul_f32 v[8:9], v[8:9], v[16:17] op_sel_hi:[1,0]\n"
    "v_pk_mul_f32 v[10:11], v[10:11], v[16:17] op_sel_hi:[1,0]\n"
    "v_pk_mul_f32 v[12:13], v[12:13], v[16:17] op_sel_hi:[1,0]\n"
    "v_pk_mul_f32 v[14:15], v[14:15], v[16:17] op_sel_hi:[1,0]\n"
    "v_cvt_pk_bf16_f32 v4, v8, v9\n"
    "v_cvt_pk_bf16_f32 v5, v10, v11\n"
    "v_cvt_pk_bf16_f32 v6, v12, v13\n"
    "v_cvt_pk_bf16_f32 v7, v14, v15\n"
    "s_mov_b64 exec, vcc\n"
    "ds_write_b128 v2, v[4:7] offset:512\n"
    "s_mov_b64 exec, 1\n"
    "v_mov_b32 v8, 1\n"
    "ds_add_u32 v0, v8 offset:0\n"
    "s_mov_b64 exec, -1\n"
    "s_lshl_b32 s53, s53, 16\n"
    "s_add_u32 s40, s8, s53\n"
    "s_addc_u32 s41, s9, 0\n"
    "s_and_b32 s41, s41, 0xffff\n"
    "s_mov_b32 s42, 0x10000\n"
    "s_mov_b32 s43, 0x20000\n"
    "s_mov_b32 s32, s22\n"
    "s_and_b32 s33, s23, 0xffff\n"
    "s_mov_b32 s34, 1712128\n"
    "s_mov_b32 s35, 0x20000\n"
    "s_mov_b32 s50, 0xfffffff0\n"
    "s_mov_b32 s51, 0x05040100\n"
    "s_mov_b32 s52, 0x07060302\n"
    "s_lshl_b32 s58, s29, 12\n"
    "s_and_b32 s58, s58, 0xffff\n"
    "v_lshrrev_b32 v2, 3, v1\n"
    "v_and_b32 v5, 7, v1\n"
    "v_lshlrev_b32 v3, 4, v5\n"
    "v_lshl_add_u32 v3, v2, 9, v3\n"
    "s_lshl_b32 s54, s30, 7\n"
    "v_add_u32 v3, s54, v3\n"
    "s_lshl_b32 s59, s30, 2\n"
    "v_mov_b32 v150, s59\n"
    "v_lshrrev_b32 v6, 4, v1\n"
    "v_and_b32 v7, 1, v1\n"
    "v_lshlrev_b32 v151, 6, v6\n"
    "v_lshl_add_u32 v151, v7, 5, v151\n"
    "v_bfe_u32 v7, v1, 2, 1\n"
    "v_lshl_add_u32 v151, v7, 8, v151\n"
    "s_lshl3_add_u32 s59, s25, s30\n"
    "s_lshl2_add_u32 s59, s59, s27\n"
    "s_lshl3_add_u32 s59, s59, s28\n"
    "s_lshl_b32 s59, s59, 8\n"
    "s_add_u32 s59, s59, 327680\n"
    "v_lshlrev_b32 v148, 15, v6\n"
    "v_lshl_add_u32 v148, v5, 5, v148\n"
    "v_bfe_u32 v7, v1, 3, 1\n"
    "v_lshl_add_u32 v148, v7, 4, v148\n"
    "v_add_u32 v148, s59, v148\n"
    "s_lshl2_add_u32 s56, s26, s30\n"
    "s_bitcmp1_b32 s56, 0\n"
    "s_cselect_b32 s44, s16, s14\n"
    "s_cselect_b32 s45, s17, s15\n"
    "s_mov_b32 s46, 1605632\n"
    "s_mov_b32 s47, 0x20000\n"
    "s_lshr_b32 s57, s56, 1\n"
    "s_mul_i32 s57, s57, 28\n"
    "v_add_u32 v4, s57, v1\n"
    "v_lshrrev_b32 v5, 2, v4\n"
    "v_mul_u32_u24 v5, 3584, v5\n"
    "v_and_b32 v4, 3, v4\n"
    "v_lshl_add_u32 v4, v4, 7, v5\n"
    "s_mul_i32 s57, s25, 448\n"
    "s_andn2_b32 s57, s57, 127\n"
    "v_add_u32 v152, s57, v4\n"
    "s_mul_i32 s56, s56, 13\n"
    "v_add_u32 v153, s56, v1\n"
    "v_lshlrev_b32 v153, 7, v153\n"
    "s_mul_i32 s56, s25, 200704\n"
    "s_add_u32 s36, s18, s56\n"
    "s_addc_u32 s37, s19, 0\n"
    "s_and_b32 s37, s37, 0xffff\n"
    "s_mov_b32 s38, 200704\n"
    "s_mov_b32 s39, 0x20000\n"
    "s_lshl2_add_u32 s56, s26, s30\n"
    "s_mul_i32 s56, s56, 28\n"
    "v_add_u32 v154, s56, v1\n"
    "v_lshlrev_b32 v154, 7, v154\n"
    "s_xor_b32 s3, s25, 4\n"
    "s_mul_i32 s56, s3, 458752\n"
    "s_add_u32 s4, s12, s56\n"
    "s_addc_u32 s5, s13, 0\n"
    "s_and_b32 s5, s5, 0xffff\n"
    "s_mov_b32 s6, 458752\n"
    "s_mov_b32 s7, 0x20000\n"
    "s_mov_b32 s63, 0x400000\n"
    ".Lfk_sp5:\n"
    "ds_read_b32 v5, v150 offset:48\n"
    "s_sub_u32 s63, s63, 1\n"
    "s_cbranch_scc1 .Lfk_so6\n"
    "s_waitcnt lgkmcnt(0)\n"
    "v_cmp_gt_u32 vcc, 1, v5\n"
    "s_cbranch_vccz .Lfk_so6\n"
    "s_sleep 1\n"
    "s_branch .Lfk_sp5\n"
    ".Lfk_so6:\n"
    "s_waitcnt lgkmcnt(0)\n"
    "buffer_load_dwordx4 v[8:11], v3, s[40:43], s58 offen nt\n"
    "s_add_u32 s58, s58, 0x1000\n"
    "s_and_b32 s58, s58, 0xffff\n"
    ".Lfk_sp7:\n"
    "ds_read_b32 v5, v150 offset:48\n"
    "s_sub_u32 s63, s63, 1\n"
    "s_cbranch_scc1 .Lfk_so8\n"
    "s_waitcnt lgkmcnt(0)\n"
    "v_cmp_gt_u32 vcc, 2, v5\n"
    "s_cbranch_vccnz .Lfk_sp7\n"
    ".Lfk_so8:\n"
    "s_waitcnt lgkmcnt(0)\n"
    "buffer_load_dwordx4 v[12:15], v3, s[40:43], s58 offen nt\n"
    "s_add_u32 s58, s58, 0x1000\n"
    "s_and_b32 s58, s58, 0xffff\n"
    ".Lfk_sp9:\n"
    "ds_read_b32 v5, v150 offset:48\n"
    "s_sub_u32 s63, s63, 1\n"
    "s_cbranch_scc1 .Lfk_so10\n"
    "s_waitcnt lgkmcnt(0)\n"
    "v_cmp_gt_u32 vcc, 3, v5\n"
    "s_cbranch_vccnz .Lfk_sp9\n"
    ".Lfk_so10:\n"
    "s_waitcnt lgkmcnt(0)\n"
    "buffer_load_dwordx4 v[16:19], v3, s[40:43], s58 offen nt\n"
    "s_add_u32 s58, s58, 0x1000\n"
    "s_and_b32 s58, s58, 0xffff\n"
    ".Lfk_sp11:\n"
    "ds_read_b32 v5, v150 offset:48\n"
    "s_sub_u32 s63, s63, 1\n"
    "s_cbranch_scc1 .Lfk_so12\n"
    "s_waitcnt lgkmcnt(0)\n"
    "v_cmp_gt_u32 vcc, 4, v5\n"
    "s_cbranch_vccnz .Lfk_sp11\n"
    ".Lfk_so12:\n"
    "s_waitcnt lgkmcnt(0)\n"
    "buffer_load_dwordx4 v[20:23], v3, s[40:43], s58 offen nt\n"
    "s_add_u32 s58, s58, 0x1000\n"
    "s_and_b32 s58, s58, 0xffff\n"
    ".Lfk_sp13:\n"
    "ds_read_b32 v5, v150 offset:48\n"
    "s_sub_u32 s63, s63, 1\n"
    "s_cbranch_scc1 .Lfk_so14\n"
    "s_waitcnt lgkmcnt(0)\n"
    "v_cmp_gt_u32 vcc, 5, v5\n"
    "s_cbranch_vccnz .Lfk_sp13\n"
    ".Lfk_so14:\n"
    "s_waitcnt lgkmcnt(0)\n"
    "buffer_load_dwordx4 v[24:27], v3, s[40:43], s58 offen nt\n"
    "s_add_u32 s58, s58, 0x1000\n"
    "s_and_b32 s58, s58, 0xffff\n"
    ".Lfk_sp15:\n"
    "ds_read_b32 v5, v150 offset:48\n"
    "s_sub_u32 s63, s63, 1\n"
    "s_cbranch_scc1 .Lfk_so16\n"
    "s_waitcnt lgkmcnt(0)\n"
    "v_cmp_gt_u32 vcc, 6, v5\n"
    "s_cbranch_vccnz .Lfk_sp15\n"
    ".Lfk_so16:\n"
    "s_waitcnt lgkmcnt(0)\n"
    "buffer_load_dwordx4 v[28:31], v3, s[40:43], s58 offen nt\n"
    "s_add_u32 s58, s58, 0x1000\n"
    "s_and_b32 s58, s58, 0xffff\n"
    "s_waitcnt vmcnt(5)\n"
    ".Lfk_pc17:\n"
    "s_memrealtime s[18:19]\n"
    "s_waitcnt lgkmcnt(0)\n"
    "s_sub_u32 s18, s18, s60\n"
    "s_cmp_lt_i32 s18, 330\n"
    "s_cbranch_scc1 .Lfk_pc17\n"
    "buffer_load_dwordx4 v[32:35], v3, s[40:43], s58 offen nt\n"
    "s_add_u32 s58, s58, 0x1000\n"
    "s_and_b32 s58, s58, 0xffff\n"
    "s_waitcnt vmcnt(5)\n"
    ".Lfk_pc18:\n"
    "s_memrealtime s[18:19]\n"
    "s_waitcnt lgkmcnt(0)\n"
    "s_sub_u32 s18, s18, s60\n"
    "s_cmp_lt_i32 s18, 345\n"
    "s_cbranch_scc1 .Lfk_pc18\n"
    "buffer_load_dwordx4 v[36:39], v3, s[40:43], s58 offen nt\n"
    "s_add_u32 s58, s58, 0x1000\n"
    "s_and_b32 s58, s58, 0xffff\n"
    "v_perm_b32 v72, v12, v8, s51\n"
    "v_perm_b32 v76, v12, v8, s52\n"
    "v_perm_b32 v80, v13, v9, s51\n"
    "v_perm_b32 v84, v13, v9, s52\n"
    "v_perm_b32 v88, v14, v10, s51\n"
    "v_perm_b32 v92, v14, v10, s52\n"
    "v_perm_b32 v96, v15, v11, s51\n"
    "v_perm_b32 v100, v15, v11, s52\n"
    "s_waitcnt vmcnt(5)\n"
    ".Lfk_pc19:\n"
    "s_memrealtime s[18:19]\n"
    "s_waitcnt lgkmcnt(0)\n"
    "s_sub_u32 s18, s18, s60\n"
    "s_cmp_lt_i32 s18, 360\n"
    "s_cbranch_scc1 .Lfk_pc19\n"
    "buffer_load_dwordx4 v[40:43], v3, s[40:43], s58 offen nt\n"
    "s_add_u32 s58, s58, 0x1000\n"
    "s_and_b32 s58, s58, 0xffff\n"
    "s_waitcnt vmcnt(5)\n"
    ".Lfk_pc20:\n"
    "s_memrealtime s[18:19]\n"
    "s_waitcnt lgkmcnt(0)\n"
    "s_sub_u32 s18, s18, s60\n"
    "s_cmp_lt_i32 s18, 375\n"
    "s_cbranch_scc1 .Lfk_pc20\n"
    "buffer_load_dwordx4 v[44:47], v3, s[40:43], s58 offen nt\n"
    "s_add_u32 s58, s58, 0x1000\n"
    "s_and_b32 s58, s58, 0xffff\n"
    "v_perm_b32 v73, v20, v16, s51\n"
    "v_perm_b32 v77, v20, v16, s52\n"
    "v_perm_b32 v81, v21, v17, s51\n"
    "v_perm_b32 v85, v21, v17, s52\n"
    "v_perm_b32 v89, v22, v18, s51\n"
    "v_perm_b32 v93, v22, v18, s52\n"
    "v_perm_b32 v97, v23, v19, s51\n"
    "v_perm_b32 v101, v23, v19, s52\n"
    "s_waitcnt vmcnt(5)\n"
    ".Lfk_pc21:\n"
    "s_memrealtime s[18:19]\n"
    "s_waitcnt lgkmcnt(0)\n"
    "s_sub_u32 s18, s18, s60\n"
    "s_cmp_lt_i32 s18, 390\n"
    "s_cbranch_scc1 .Lfk_pc21\n"
    "buffer_load_dwordx4 v[48:51], v3, s[40:43], s58 offen nt\n"
    "s_add_u32 s58, s58, 0x1000\n"
    "s_and_b32 s58, s58, 0xffff\n"
    "s_waitcnt vmcnt(5)\n"
    ".Lfk_pc22:\n"
    "s_memrealtime s[18:19]\n"
    "s_waitcnt lgkmcnt(0)\n"
    "s_sub_u32 s18, s18, s60\n"
    "s_cmp_lt_i32 s18, 405\n"
    "s_cbranch_scc1 .Lfk_pc22\n"
    "buffer_load_dwordx4 v[52:55], v3, s[40:43], s58 offen nt\n"
    "s_add_u32 s58, s58, 0x1000\n"
    "s_and_b32 s58, s58, 0xffff\n"
    "v_perm_b32 v74, v28, v24, s51\n"
    "v_perm_b32 v78, v28, v24, s52\n"
    "v_perm_b32 v82, v29, v25, s51\n"
    "v_perm_b32 v86, v29, v25, s52\n"
    "v_perm_b32 v90, v30, v26, s51\n"
    "v_perm_b32 v94, v30, v26, s52\n"
    "v_perm_b32 v98, v31, v27, s51\n"
    "v_perm_b32 v102, v31, v27, s52\n"
    "s_waitcnt vmcnt(5)\n"
    ".Lfk_pc23:\n"
    "s_memrealtime s[18:19]\n"
    "s_waitcnt lgkmcnt(0)\n"
    "s_sub_u32 s18, s18, s60\n"
    "s_cmp_lt_i32 s18, 420\n"
    "s_cbranch_scc1 .Lfk_pc23\n"
    "buffer_load_dwordx4 v[56:59], v3, s[40:43], s58 offen nt\n"
    "s_add_u32 s58, s58, 0x1000\n"
    "s_and_b32 s58, s58, 0xffff\n"
    "s_waitcnt vmcnt(5)\n"
    ".Lfk_pc24:\n"
    "s_memrealtime s[18:19]\n"
    "s_waitcnt lgkmcnt(0)\n"
    "s_sub_u32 s18, s18, s60\n"
    "s_cmp_lt_i32 s18, 435\n"
    "s_cbranch_scc1 .Lfk_pc24\n"
    "buffer_load_dwordx4 v[60:63], v3, s[40:43], s58 offen nt\n"
    "s_add_u32 s58, s58, 0x1000\n"
    "s_and_b32 s58, s58, 0xffff\n"
    "v_perm_b32 v75, v36, v32, s51\n"
    "v_perm_b32 v79, v36, v32, s52\n"
    "v_perm_b32 v83, v37, v33, s51\n"
    "v_perm_b32 v87, v37, v33, s52\n"
    "v_perm_b32 v91, v38, v34, s51\n"
    "v_perm_b32 v95, v38, v34, s52\n"
    "v_perm_b32 v99, v39, v35, s51\n"
    "v_perm_b32 v103, v39, v35, s52\n"
    "s_waitcnt vmcnt(5)\n"
    ".Lfk_pc25:\n"
    "s_memrealtime s[18:19]\n"
    "s_waitcnt lgkmcnt(0)\n"
    "s_sub_u32 s18, s18, s60\n"
    "s_cmp_lt_i32 s18, 450\n"
    "s_cbranch_scc1 .Lfk_pc25\n"
    "buffer_load_dwordx4 v[64:67], v3, s[40:43], s58 offen nt\n"
    "s_add_u32 s58, s58, 0x1000\n"
    "s_and_b32 s58, s58, 0xffff\n"
    "s_waitcnt vmcnt(5)\n"
    ".Lfk_pc26:\n"
    "s_memrealtime s[18:19]\n"
    "s_waitcnt lgkmcnt(0)\n"
    "s_sub_u32 s18, s18, s60\n"
    "s_cmp_lt_i32 s18, 465\n"
    "s_cbranch_scc1 .Lfk_pc26\n"
    "buffer_load_dwordx4 v[68:71], v3, s[40:43], s58 offen nt\n"
    "v_perm_b32 v104, v44, v40, s51\n"
    "v_perm_b32 v108, v44, v40, s52\n"
    "v_perm_b32 v112, v45, v41, s51\n"
    "v_perm_b32 v116, v45, v41, s52\n"
    "v_perm_b32 v120, v46, v42, s51\n"
    "v_perm_b32 v124, v46, v42, s52\n"
    "v_perm_b32 v128, v47, v43, s51\n"
    "v_perm_b32 v132, v47, v43, s52\n"
    "s_waitcnt vmcnt(5)\n"
    ".Lfk_pc27:\n"
    "s_memrealtime s[18:19]\n"
    "s_waitcnt lgkmcnt(0)\n"
    "s_sub_u32 s18, s18, s60\n"
    "s_cmp_lt_i32 s18, 480\n"
    "s_cbranch_scc1 .Lfk_pc27\n"
    "s_mov_b64 exec, 0xfffffff\n"
    "buffer_load_dword v154, v154, s[4:7], 0 offen\n"
    "s_mov_b64 exec, -1\n"
    "s_mov_b64 exec, 1\n"
    "v_mov_b32 v5, 1\n"
    "ds_add_u32 v0, v5 offset:24\n"
    "s_mov_b64 exec, -1\n"
    "s_mov_b32 s63, 0x400000\n"
    ".Lfk_sp28:\n"
    "ds_read_b32 v5, v0 offset:20\n"
    "s_sub_u32 s63, s63, 1\n"
    "s_cbranch_scc1 .Lfk_so29\n"
    "s_waitcnt lgkmcnt(0)\n"
    "v_cmp_gt_u32 vcc, 4, v5\n"
    "s_cbranch_vccnz .Lfk_sp28\n"
    ".Lfk_so29:\n"
    "s_waitcnt lgkmcnt(0)\n"
    "ds_read_b128 v[136:139], v151 offset:5120\n"
    "ds_read_b128 v[140:143], v151 offset:5136\n"
    "ds_read_b32 v6, v0 offset:64\n"
    "s_waitcnt lgkmcnt(0)\n"
    "v_readfirstlane_b32 s49, v6\n"
    "s_nop 0\n"
    "s_and_b32 s48, s49, 15\n"
    "v_mov_b32 v149, s48\n"
    "v_mfma_f32_16x16x32_bf16 v[8:11], v[136:139], v[72:75], 0\n"
    "v_mfma_f32_16x16x32_bf16 v[12:15], v[136:139], v[76:79], 0\n"
    "v_mfma_f32_16x16x32_bf16 v[16:19], v[136:139], v[80:83], 0\n"
    "v_mfma_f32_16x16x32_bf16 v[20:23], v[136:139], v[84:87], 0\n"
    "v_mfma_f32_16x16x32_bf16 v[24:27], v[136:139], v[88:91], 0\n"
    "v_mfma_f32_16x16x32_bf16 v[28:31], v[136:139], v[92:95], 0\n"
    "v_mfma_f32_16x16x32_bf16 v[32:35], v[136:139], v[96:99], 0\n"
    "v_mfma_f32_16x16x32_bf16 v[36:39], v[136:139], v[100:103], 0\n"
    "s_waitcnt vmcnt(5)\n"
    "v_perm_b32 v105, v52, v48, s51\n"
    "v_perm_b32 v109, v52, v48, s52\n"
    "v_perm_b32 v113, v53, v49, s51\n"
    "v_perm_b32 v117, v53, v49, s52\n"
    "v_perm_b32 v121, v54, v50, s51\n"
    "v_perm_b32 v125, v54, v50, s52\n"
    "v_perm_b32 v129, v55, v51, s51\n"
    "v_perm_b32 v133, v55, v51, s52\n"
    "s_waitcnt vmcnt(3)\n"
    "v_perm_b32 v106, v60, v56, s51\n"
    "v_perm_b32 v110, v60, v56, s52\n"
    "v_perm_b32 v114, v61, v57, s51\n"
    "v_perm_b32 v118, v61, v57, s52\n"
    "v_perm_b32 v122, v62, v58, s51\n"
    "v_perm_b32 v126, v62, v58, s52\n"
    "v_perm_b32 v130, v63, v59, s51\n"
    "v_perm_b32 v134, v63, v59, s52\n"
    "s_waitcnt vmcnt(1)\n"
    "v_perm_b32 v107, v68, v64, s51\n"
    "v_perm_b32 v123, v70, v66, s51\n"
    "v_perm_b32 v111, v68, v64, s52\n"
    "v_mfma_f32_16x16x32_bf16 v[8:11], v[140:143], v[104:107], v[8:11]\n"
    "v_perm_b32 v127, v70, v66, s52\n"
    "v_mfma_f32_16x16x32_bf16 v[24:27], v[140:143], v[120:123], v[24:27]\n"
    "v_perm_b32 v115, v69, v65, s51\n"
    "v_mfma_f32_16x16x32_bf16 v[12:15], v[140:143], v[108:111], v[12:15]\n"
    "v_perm_b32 v131, v71, v67, s51\n"
    "v_mfma_f32_16x16x32_bf16 v[28:31], v[140:143], v[124:127], v[28:31]\n"
    "v_perm_b32 v119, v69, v65, s52\n"
    "v_mfma_f32_16x16x32_bf16 v[16:19], v[140:143], v[112:115], v[16:19]\n"
    "v_perm_b32 v135, v71, v67, s52\n"
    "v_mfma_f32_16x16x32_bf16 v[32:35], v[140:143], v[128:131], v[32:35]\n"
    "v_mfma_f32_16x16x32_bf16 v[20:23], v[140:143], v[116:119], v[20:23]\n"
    "v_mfma_f32_16x16x32_bf16 v[36:39], v[140:143], v[132:135], v[36:39]\n"
    "v_add_f32_dpp v144, v9, v8 row_ror:8 row_mask:0xf bank_mask:0x3\n"
    "s_nop 1\n"
    "v_add_f32_dpp v144, v24, v25 row_ror:8 row_mask:0xf bank_mask:0xc\n"
    "v_and_or_b32 v144, v144, s50, v149\n"
    "v_add_f32_dpp v145, v13, v12 row_ror:8 row_mask:0xf bank_mask:0x3\n"
    "s_nop 1\n"
    "v_add_f32_dpp v145, v28, v29 row_ror:8 row_mask:0xf bank_mask:0xc\n"
    "v_and_or_b32 v145, v145, s50, v149\n"
    "v_add_f32_dpp v146, v17, v16 row_ror:8 row_mask:0xf bank_mask:0x3\n"
    "s_nop 1\n"
    "v_add_f32_dpp v146, v32, v33 row_ror:8 row_mask:0xf bank_mask:0xc\n"
    "v_and_or_b32 v146, v146, s50, v149\n"
    "v_add_f32_dpp v147, v21, v20 row_ror:8 row_mask:0xf bank_mask:0x3\n"
    "s_nop 1\n"
    "v_add_f32_dpp v147, v36, v37 row_ror:8 row_mask:0xf bank_mask:0xc\n"
    "v_and_or_b32 v147, v147, s50, v149\n"
    "s_mov_b32 exec_hi, 0\n"
    "buffer_store_dwordx4 v[144:147], v148, s[32:35], 0 offen sc1\n"
    "s_mov_b64 exec, 1\n"
    "v_mov_b32 v5, 1\n"
    "ds_add_u32 v0, v5 offset:28\n"
    "s_mov_b64 exec, -1\n"
    "s_mov_b64 exec, 0xfffffff\n"
    "buffer_load_dword v152, v152, s[44:47], 0 offen\n"
    "s_mov_b64 exec, 0x1fff\n"
    "buffer_load_dword v153, v153, s[36:39], 0 offen\n"
    "s_mov_b64 exec, -1\n"
    "s_mov_b64 exec, 1\n"
    "v_mov_b32 v5, 1\n"
    "ds_add_u32 v0, v5 offset:36\n"
    "s_mov_b64 exec, -1\n"
    ".Lfk_done:\n"
    "s_endpgm\n"
    ".Lfk_Q:\n"
    "s_setprio 3\n"
    "s_lshr_b32 s26, s2, 3\n"
    "s_mov_b32 s50, 0xfffffff0\n"
    "s_mov_b32 s51, 0x05040100\n"
    "s_mov_b32 s52, 0x07060302\n"
    "s_mov_b32 s32, s22\n"
    "s_and_b32 s33, s23, 0xffff\n"
    "s_mov_b32 s34, 1712128\n"
    "s_mov_b32 s35, 0x20000\n"
    "v_mov_b32 v99, 0\n"
    "v_mov_b32 v103, 0\n"
    "v_mov_b32 v107, 0\n"
    "v_mov_b32 v111, 0\n"
    "v_mov_b32 v115, 0\n"
    "v_mov_b32 v119, 0\n"
    "v_mov_b32 v123, 0\n"
    "v_mov_b32 v127, 0\n"
    "v_mov_b32 v135, 0\n"
    "v_bfe_u32 v2, v1, 2, 2\n"
    "v_lshrrev_b32 v4, 4, v1\n"
    "v_mul_u32_u24 v2, 896, v2\n"
    "v_and_b32 v5, 1, v1\n"
    "s_mul_i32 s53, s27, 224\n"
    "v_lshl_add_u32 v5, v4, 1, v5\n"
    "v_mad_u32_u24 v2, v5, 28, v2\n"
    "v_add_u32 v2, s53, v2\n"
    "s_lshl_b32 s53, s3, 15\n"
    "s_lshl_b32 s54, s27, 11\n"
    "s_lshl_b32 s55, s28, 8\n"
    "s_add_u32 s53, s53, s54\n"
    "s_add_u32 s53, s53, s55\n"
    "s_add_u32 s53, s53, 65536\n"
    "v_lshlrev_b32 v4, 13, v4\n"
    "v_lshl_add_u32 v3, v3, 5, v4\n"
    "v_bfe_u32 v4, v1, 3, 1\n"
    "v_lshl_add_u32 v3, v4, 4, v3\n"
    "v_add_u32 v3, s53, v3\n"
    "s_mov_b32 s63, 0x400000\n"
    ".Lfk_sp30:\n"
    "ds_read_b32 v4, v0 offset:4\n"
    "s_sub_u32 s63, s63, 1\n"
    "s_cbranch_scc1 .Lfk_so31\n"
    "s_waitcnt lgkmcnt(0)\n"
    "v_cmp_gt_u32 vcc, 1, v4\n"
    "s_cbranch_vccnz .Lfk_sp30\n"
    ".Lfk_so31:\n"
    "s_waitcnt lgkmcnt(0)\n"
    "ds_read_b32 v4, v0 offset:64\n"
    "s_waitcnt lgkmcnt(0)\n"
    "v_readfirstlane_b32 s49, v4\n"
    "s_nop 0\n"
    "s_and_b32 s48, s49, 15\n"
    "s_waitcnt vmcnt(12)\n"
    "v_perm_b32 v64, v12, v8, s51\n"
    "v_perm_b32 v68, v12, v8, s52\n"
    "v_perm_b32 v72, v13, v9, s51\n"
    "v_perm_b32 v76, v13, v9, s52\n"
    "v_perm_b32 v80, v14, v10, s51\n"
    "v_perm_b32 v84, v14, v10, s52\n"
    "v_perm_b32 v88, v15, v11, s51\n"
    "v_perm_b32 v92, v15, v11, s52\n"
    "s_waitcnt vmcnt(10)\n"
    "v_perm_b32 v65, v20, v16, s51\n"
    "v_perm_b32 v69, v20, v16, s52\n"
    "v_perm_b32 v73, v21, v17, s51\n"
    "v_perm_b32 v77, v21, v17, s52\n"
    "v_perm_b32 v81, v22, v18, s51\n"
    "v_perm_b32 v85, v22, v18, s52\n"
    "v_perm_b32 v89, v23, v19, s51\n"
    "v_perm_b32 v93, v23, v19, s52\n"
    "s_waitcnt vmcnt(8)\n"
    "v_perm_b32 v66, v28, v24, s51\n"
    "v_perm_b32 v70, v28, v24, s52\n"
    "v_perm_b32 v74, v29, v25, s51\n"
    "v_perm_b32 v78, v29, v25, s52\n"
    "v_perm_b32 v82, v30, v26, s51\n"
    "v_perm_b32 v86, v30, v26, s52\n"
    "v_perm_b32 v90, v31, v27, s51\n"
    "v_perm_b32 v94, v31, v27, s52\n"
    "s_waitcnt vmcnt(6)\n"
    "v_perm_b32 v67, v36, v32, s51\n"
    "v_perm_b32 v71, v36, v32, s52\n"
    "v_perm_b32 v75, v37, v33, s51\n"
    "v_perm_b32 v79, v37, v33, s52\n"
    "v_perm_b32 v83, v38, v34, s51\n"
    "v_perm_b32 v87, v38, v34, s52\n"
    "v_perm_b32 v91, v39, v35, s51\n"
    "v_perm_b32 v95, v39, v35, s52\n"
    "s_mov_b32 s63, 0x400000\n"
    ".Lfk_sp32:\n"
    "ds_read_b32 v4, v0 offset:0\n"
    "s_sub_u32 s63, s63, 1\n"
    "s_cbranch_scc1 .Lfk_so33\n"
    "s_waitcnt lgkmcnt(0)\n"
    "v_cmp_gt_u32 vcc, 4, v4\n"
    "s_cbranch_vccnz .Lfk_sp32\n"
    ".Lfk_so33:\n"
    "s_waitcnt lgkmcnt(0)\n"
    "ds_read2_b32 v[128:129], v2 offset0:128 offset1:129\n"
    "ds_read2_b32 v[130:131], v2 offset0:130 offset1:131\n"
    "ds_read2_b32 v[132:133], v2 offset0:132 offset1:133\n"
    "ds_read_b32 v134, v2 offset:536\n"
    "v_mov_b32 v8, s48\n"
    "s_waitcnt vmcnt(4)\n"
    "v_perm_b32 v96, v44, v40, s51\n"
    "v_perm_b32 v100, v44, v40, s52\n"
    "v_perm_b32 v104, v45, v41, s51\n"
    "v_perm_b32 v108, v45, v41, s52\n"
    "v_perm_b32 v112, v46, v42, s51\n"
    "v_perm_b32 v116, v46, v42, s52\n"
    "v_perm_b32 v120, v47, v43, s51\n"
    "v_perm_b32 v124, v47, v43, s52\n"
    "s_waitcnt lgkmcnt(2)\n"
    "v_mfma_f32_16x16x32_bf16 v[136:139], v[128:131], v[64:67], 0\n"
    "v_mfma_f32_16x16x32_bf16 v[152:155], v[128:131], v[80:83], 0\n"
    "v_mfma_f32_16x16x32_bf16 v[140:143], v[128:131], v[68:71], 0\n"
    "v_mfma_f32_16x16x32_bf16 v[156:159], v[128:131], v[84:87], 0\n"
    "v_mfma_f32_16x16x32_bf16 v[144:147], v[128:131], v[72:75], 0\n"
    "v_mfma_f32_16x16x32_bf16 v[160:163], v[128:131], v[88:91], 0\n"
    "v_mfma_f32_16x16x32_bf16 v[148:151], v[128:131], v[76:79], 0\n"
    "v_mfma_f32_16x16x32_bf16 v[164:167], v[128:131], v[92:95], 0\n"
    "s_waitcnt vmcnt(2)\n"
    "v_perm_b32 v97, v52, v48, s51\n"
    "v_perm_b32 v101, v52, v48, s52\n"
    "v_perm_b32 v105, v53, v49, s51\n"
    "v_perm_b32 v109, v53, v49, s52\n"
    "v_perm_b32 v113, v54, v50, s51\n"
    "v_perm_b32 v117, v54, v50, s52\n"
    "v_perm_b32 v121, v55, v51, s51\n"
    "v_perm_b32 v125, v55, v51, s52\n"
    "s_waitcnt vmcnt(0)\n"
    "v_perm_b32 v98, v60, v56, s51\n"
    "v_perm_b32 v102, v60, v56, s52\n"
    "v_perm_b32 v106, v61, v57, s51\n"
    "v_perm_b32 v110, v61, v57, s52\n"
    "v_perm_b32 v114, v62, v58, s51\n"
    "v_perm_b32 v118, v62, v58, s52\n"
    "v_perm_b32 v122, v63, v59, s51\n"
    "v_perm_b32 v126, v63, v59, s52\n"
    "s_waitcnt lgkmcnt(0)\n"
    "v_mfma_f32_16x16x32_bf16 v[136:139], v[132:135], v[96:99], v[136:139]\n"
    "v_mfma_f32_16x16x32_bf16 v[152:155], v[132:135], v[112:115], v[152:155]\n"
    "v_mfma_f32_16x16x32_bf16 v[140:143], v[132:135], v[100:103], v[140:143]\n"
    "v_mfma_f32_16x16x32_bf16 v[156:159], v[132:135], v[116:119], v[156:159]\n"
    "v_mfma_f32_16x16x32_bf16 v[144:147], v[132:135], v[104:107], v[144:147]\n"
    "v_mfma_f32_16x16x32_bf16 v[160:163], v[132:135], v[120:123], v[160:163]\n"
    "v_mfma_f32_16x16x32_bf16 v[148:151], v[132:135], v[108:111], v[148:151]\n"
    "v_mfma_f32_16x16x32_bf16 v[164:167], v[132:135], v[124:127], v[164:167]\n"
    "s_nop 0\n"
    "v_add_f32_dpp v4, v137, v136 row_ror:8 row_mask:0xf bank_mask:0x3\n"
    "s_nop 1\n"
    "v_add_f32_dpp v4, v152, v153 row_ror:8 row_mask:0xf bank_mask:0xc\n"
    "v_add_u32 v4, 8, v4\n"
    "v_and_or_b32 v4, v4, s50, v8\n"
    "v_add_f32_dpp v5, v141, v140 row_ror:8 row_mask:0xf bank_mask:0x3\n"
    "s_nop 1\n"
    "v_add_f32_dpp v5, v156, v157 row_ror:8 row_mask:0xf bank_mask:0xc\n"
    "v_add_u32 v5, 8, v5\n"
    "v_and_or_b32 v5, v5, s50, v8\n"
    "v_add_f32_dpp v6, v145, v144 row_ror:8 row_mask:0xf bank_mask:0x3\n"
    "s_nop 1\n"
    "v_add_f32_dpp v6, v160, v161 row_ror:8 row_mask:0xf bank_mask:0xc\n"
    "v_add_u32 v6, 8, v6\n"
    "v_and_or_b32 v6, v6, s50, v8\n"
    "v_add_f32_dpp v7, v149, v148 row_ror:8 row_mask:0xf bank_mask:0x3\n"
    "s_nop 1\n"
    "v_add_f32_dpp v7, v164, v165 row_ror:8 row_mask:0xf bank_mask:0xc\n"
    "v_add_u32 v7, 8, v7\n"
    "v_and_or_b32 v7, v7, s50, v8\n"
    "buffer_store_dwordx4 v[4:7], v3, s[32:35], 0 offen sc1\n"
    "s_mov_b64 exec, 1\n"
    "v_mov_b32 v9, 1\n"
    "ds_add_u32 v0, v9 offset:8\n"
    "s_mov_b64 exec, -1\n"
    "s_branch .Lfk_tail\n"
    ".Lfk_EG:\n"
    "s_setprio 3\n"
    "s_lshr_b32 s26, s2, 3\n"
    "s_mov_b32 s32, s22\n"
    "s_and_b32 s33, s23, 0xffff\n"
    "s_mov_b32 s34, 1712128\n"
    "s_mov_b32 s35, 0x20000\n"
    "s_cmp_lg_u32 s24, 1\n"
    "s_cbranch_scc1 .Lfk_G\n"
    "s_waitcnt vmcnt(0)\n"
    "v_add_u32 v4, 1, v4\n"
    "ds_write_b32 v0, v4 offset:64\n"
    "s_mov_b64 exec, 1\n"
    "v_mov_b32 v5, 1\n"
    "ds_add_u32 v0, v5 offset:4\n"
    "s_mov_b64 exec, -1\n"
    "s_mov_b32 s50, 0xfffffff0\n"
    "s_mov_b32 s51, 0x05040100\n"
    "s_mov_b32 s52, 0x07060302\n"
    "v_and_b32 v7, 15, v4\n"
    "v_bfe_u32 v2, v1, 1, 1\n"
    "v_readfirstlane_b32 s48, v7\n"
    "v_lshlrev_b32 v2, 4, v2\n"
    "s_lshl_b32 s53, s25, 5\n"
    "s_add_u32 s53, s53, s26\n"
    "s_lshl_b32 s53, s53, 7\n"
    "s_add_u32 s61, s53, 4096\n"
    "v_lshlrev_b32 v3, 2, v1\n"
    "s_cmp_ge_u32 s26, 7\n"
    "s_cselect_b32 s30, 1, 0\n"
    "s_xor_b32 s3, s25, 4\n"
    "s_lshl1_add_u32 s53, s3, s30\n"
    "s_lshl_b32 s53, s53, 11\n"
    "s_add_u32 s53, s53, 4096\n"
    "v_lshrrev_b32 v4, 2, v1\n"
    "v_and_b32 v5, 3, v1\n"
    "v_lshlrev_b32 v6, 7, v4\n"
    "v_lshl_add_u32 v6, v5, 4, v6\n"
    "v_add_u32 v6, s53, v6\n"
    "s_mov_b32 s63, 0x400000\n"
    ".Lfk_sp34:\n"
    "ds_read_b32 v5, v0 offset:20\n"
    "s_sub_u32 s63, s63, 1\n"
    "s_cbranch_scc1 .Lfk_so35\n"
    "s_waitcnt lgkmcnt(0)\n"
    "v_cmp_gt_u32 vcc, 4, v5\n"
    "s_cbranch_vccz .Lfk_so35\n"
    "s_sleep 2\n"
    "s_branch .Lfk_sp34\n"
    ".Lfk_so35:\n"
    "s_waitcnt lgkmcnt(0)\n"
    "ds_read_b128 v[8:11], v2 offset:128\n"
    "ds_read_b128 v[12:15], v2 offset:160\n"
    "s_mov_b32 s56, 0xaaaaaaaa\n"
    "s_mov_b32 s57, 0xaaaaaaaa\n"
    "s_waitcnt lgkmcnt(0)\n"
    "v_max3_f32 v8, v8, v9, v10\n"
    "v_add_f32 v12, v12, v13\n"
    "v_add_f32 v14, v14, v15\n"
    "v_max_f32 v8, v8, v11\n"
    "v_add_f32 v12, v12, v14\n"
    "v_cndmask_b32 v8, v8, v12, s[56:57]\n"
    "v_and_or_b32 v8, v8, s50, v7\n"
    "s_mov_b32 exec_hi, 0\n"
    "buffer_store_dword v8, v3, s[32:35], s61 offen sc1\n"
    "s_mov_b64 exec, -1\n"
    "s_cmp_ge_u32 s26, 14\n"
    "s_cbranch_scc1 .Lfk_tail\n"
    "s_mov_b32 s63, 0x400000\n"
    ".Lfk_sp36:\n"
    "ds_read_b32 v5, v0 offset:24\n"
    "s_sub_u32 s63, s63, 1\n"
    "s_cbranch_scc1 .Lfk_so37\n"
    "s_waitcnt lgkmcnt(0)\n"
    "v_cmp_gt_u32 vcc, 1, v5\n"
    "s_cbranch_vccz .Lfk_so37\n"
    "s_sleep 2\n"
    "s_branch .Lfk_sp36\n"
    ".Lfk_so37:\n"
    "s_waitcnt lgkmcnt(0)\n"
    "buffer_load_dwordx4 v[8:11], v6, s[32:35], 0 offen sc1\n"
    "s_mov_b32 s62, 0\n"
    "s_waitcnt vmcnt(0)\n"
    "v_xor_b32 v8, s48, v8\n"
    "v_xor_b32 v9, s48, v9\n"
    "v_xor_b32 v10, s48, v10\n"
    "v_xor_b32 v11, s48, v11\n"
    "v_or3_b32 v7, v8, v9, v10\n"
    "v_bitop3_b32 v7, v7, v11, 15 bitop3:0xa8\n"
    "v_cmp_ne_u32 s[60:61], 0, v7\n"
    "s_nop 0\n"
    "s_cmp_lg_u64 s[60:61], 0\n"
    "s_cbranch_scc0 .Lfk_go39\n"
    ".Lfk_g38:\n"
    "s_cmp_eq_u64 s[60:61], 0\n"
    "s_cbranch_scc1 .Lfk_s40\n"
    "buffer_load_dwordx4 v[8:11], v6, s[32:35], 0 offen sc1\n"
    ".Lfk_s40:\n"
    "s_add_u32 s62, s62, 1\n"
    "s_waitcnt vmcnt(0)\n"
    "s_cmp_eq_u64 s[60:61], 0\n"
    "s_cbranch_scc1 .Lfk_s41\n"
    "v_xor_b32 v8, s48, v8\n"
    "v_xor_b32 v9, s48, v9\n"
    "v_xor_b32 v10, s48, v10\n"
    "v_xor_b32 v11, s48, v11\n"
    "v_or3_b32 v7, v8, v9, v10\n"
    "v_bitop3_b32 v7, v7, v11, 15 bitop3:0xa8\n"
    "v_cmp_ne_u32 s[60:61], 0, v7\n"
    ".Lfk_s41:\n"
    "s_nop 0\n"
    "s_cmp_lg_u64 s[60:61], 0\n"
    "s_cbranch_scc0 .Lfk_go39\n"
    "s_cmp_lt_u32 s62, 0x20000\n"
    "s_cbranch_scc1 .Lfk_g38\n"
    ".Lfk_go39:\n"
    "v_cndmask_b32 v8, v8, v10, s[56:57]\n"
    "v_cndmask_b32 v9, v9, v11, s[56:57]\n"
    "v_and_b32 v5, 1, v1\n"
    "v_max_f32_dpp v10, v8, v8 row_ror:4 row_mask:0xf bank_mask:0xf\n"
    "s_nop 1\n"
    "v_max_f32_dpp v10, v10, v10 row_ror:8 row_mask:0xf bank_mask:0xf\n"
    "v_mov_b32 v11, v10\n"
    "s_nop 1\n"
    "v_permlane16_swap_b32 v10, v11\n"
    "v_max_f32 v10, v10, v11\n"
    "v_sub_f32 v8, v8, v10\n"
    "v_mul_f32 v8, 0x3db8aa3b, v8\n"
    "v_exp_f32 v8, v8\n"
    "v_lshlrev_b32 v4, 2, v4\n"
    "v_mul_f32 v9, v8, v9\n"
    "s_nop 1\n"
    "v_add_f32_dpp v9, v9, v9 row_ror:4 row_mask:0xf bank_mask:0xf\n"
    "s_nop 1\n"
    "v_add_f32_dpp v9, v9, v9 row_ror:8 row_mask:0xf bank_mask:0xf\n"
    "v_mov_b32 v11, v9\n"
    "s_nop 1\n"
    "v_permlane16_swap_b32 v9, v11\n"
    "v_add_f32 v9, v9, v11\n"
    "v_rcp_f32 v9, v9\n"
    "v_lshl_add_u32 v4, v5, 6, v4\n"
    "v_mul_f32 v8, v8, v9\n"
    "ds_write_b32 v4, v8 offset:192\n"
    "s_mov_b64 exec, 1\n"
    "v_mov_b32 v5, 1\n"
    "ds_add_u32 v0, v5 offset:32\n"
    "s_mov_b64 exec, -1\n"
    "s_branch .Lfk_tail\n"
    ".Lfk_G:\n"
    "s_lshl2_add_u32 s53, s25, s27\n"
    "s_lshl_b32 s53, s53, 13\n"
    "s_sub_u32 s54, s24, 2\n"
    "s_lshl_b32 s55, s54, 10\n"
    "s_add_u32 s53, s53, s55\n"
    "s_add_u32 s53, s53, 65536\n"
    "v_lshlrev_b32 v2, 4, v1\n"
    "v_add_u32 v2, s53, v2\n"
    "s_lshl_b32 s54, s54, 9\n"
    "v_lshlrev_b32 v3, 3, v1\n"
    "v_add_u32 v3, s54, v3\n"
    "s_mov_b32 s63, 0x400000\n"
    ".Lfk_sp42:\n"
    "ds_read_b32 v4, v0 offset:8\n"
    "s_sub_u32 s63, s63, 1\n"
    "s_cbranch_scc1 .Lfk_so43\n"
    "s_waitcnt lgkmcnt(0)\n"
    "v_cmp_gt_u32 vcc, 1, v4\n"
    "s_cbranch_vccz .Lfk_so43\n"
    "s_sleep 1\n"
    "s_branch .Lfk_sp42\n"
    ".Lfk_so43:\n"
    "s_waitcnt lgkmcnt(0)\n"
    "ds_read_b32 v4, v0 offset:64\n"
    "s_waitcnt lgkmcnt(0)\n"
    "v_readfirstlane_b32 s49, v4\n"
    "s_nop 0\n"
    "s_and_b32 s48, s49, 15\n"
    "s_sleep 24\n"
    "s_mov_b32 s54, 2048\n"
    "s_mov_b32 s55, 4096\n"
    "s_mov_b32 s56, 6144\n"
    "buffer_load_dwordx4 v[8:11], v2, s[32:35], 0 offen sc1\n"
    "buffer_load_dwordx4 v[12:15], v2, s[32:35], s54 offen sc1\n"
    "buffer_load_dwordx4 v[16:19], v2, s[32:35], s55 offen sc1\n"
    "buffer_load_dwordx4 v[20:23], v2, s[32:35], s56 offen sc1\n"
    "s_mov_b32 s62, 0\n"
    "s_waitcnt vmcnt(3)\n"
    "v_xor_b32 v8, s48, v8\n"
    "v_xor_b32 v9, s48, v9\n"
    "v_xor_b32 v10, s48, v10\n"
    "v_xor_b32 v11, s48, v11\n"
    "v_or3_b32 v5, v8, v9, v10\n"
    "v_bitop3_b32 v5, v5, v11, 15 bitop3:0xa8\n"
    "v_cmp_ne_u32 s[40:41], 0, v5\n"
    "s_waitcnt vmcnt(2)\n"
    "v_xor_b32 v12, s48, v12\n"
    "v_xor_b32 v13, s48, v13\n"
    "v_xor_b32 v14, s48, v14\n"
    "v_xor_b32 v15, s48, v15\n"
    "v_or3_b32 v5, v12, v13, v14\n"
    "v_bitop3_b32 v5, v5, v15, 15 bitop3:0xa8\n"
    "v_cmp_ne_u32 s[42:43], 0, v5\n"
    "s_waitcnt vmcnt(1)\n"
    "v_xor_b32 v16, s48, v16\n"
    "v_xor_b32 v17, s48, v17\n"
    "v_xor_b32 v18, s48, v18\n"
    "v_xor_b32 v19, s48, v19\n"
    "v_or3_b32 v5, v16, v17, v18\n"
    "v_bitop3_b32 v5, v5, v19, 15 bitop3:0xa8\n"
    "v_cmp_ne_u32 s[44:45], 0, v5\n"
    "s_waitcnt vmcnt(0)\n"
    "v_xor_b32 v20, s48, v20\n"
    "v_xor_b32 v21, s48, v21\n"
    "v_xor_b32 v22, s48, v22\n"
    "v_xor_b32 v23, s48, v23\n"
    "v_or3_b32 v5, v20, v21, v22\n"
    "v_bitop3_b32 v5, v5, v23, 15 bitop3:0xa8\n"
    "v_cmp_ne_u32 s[46:47], 0, v5\n"
    "s_or_b64 vcc, s[40:41], s[42:43]\n"
    "s_or_b64 vcc, vcc, s[44:45]\n"
    "s_or_b64 vcc, vcc, s[46:47]\n"
    "s_cbranch_scc0 .Lfk_go45\n"
    ".Lfk_g44:\n"
    "s_cmp_eq_u64 s[40:41], 0\n"
    "s_cbranch_scc1 .Lfk_s46\n"
    "buffer_load_dwordx4 v[8:11], v2, s[32:35], 0 offen sc1\n"
    ".Lfk_s46:\n"
    "s_cmp_eq_u64 s[42:43], 0\n"
    "s_cbranch_scc1 .Lfk_s47\n"
    "buffer_load_dwordx4 v[12:15], v2, s[32:35], s54 offen sc1\n"
    ".Lfk_s47:\n"
    "s_cmp_eq_u64 s[44:45], 0\n"
    "s_cbranch_scc1 .Lfk_s48\n"
    "buffer_load_dwordx4 v[16:19], v2, s[32:35], s55 offen sc1\n"
    ".Lfk_s48:\n"
    "s_cmp_eq_u64 s[46:47], 0\n"
    "s_cbranch_scc1 .Lfk_s49\n"
    "buffer_load_dwordx4 v[20:23], v2, s[32:35], s56 offen sc1\n"
    ".Lfk_s49:\n"
    "s_add_u32 s62, s62, 1\n"
    "s_waitcnt vmcnt(0)\n"
    "s_cmp_eq_u64 s[40:41], 0\n"
    "s_cbranch_scc1 .Lfk_s50\n"
    "v_xor_b32 v8, s48, v8\n"
    "v_xor_b32 v9, s48, v9\n"
    "v_xor_b32 v10, s48, v10\n"
    "v_xor_b32 v11, s48, v11\n"
    "v_or3_b32 v5, v8, v9, v10\n"
    "v_bitop3_b32 v5, v5, v11, 15 bitop3:0xa8\n"
    "v_cmp_ne_u32 s[40:41], 0, v5\n"
    ".Lfk_s50:\n"
    "s_cmp_eq_u64 s[42:43], 0\n"
    "s_cbranch_scc1 .Lfk_s51\n"
    "v_xor_b32 v12, s48, v12\n"
    "v_xor_b32 v13, s48, v13\n"
    "v_xor_b32 v14, s48, v14\n"
    "v_xor_b32 v15, s48, v15\n"
    "v_or3_b32 v5, v12, v13, v14\n"
    "v_bitop3_b32 v5, v5, v15, 15 bitop3:0xa8\n"
    "v_cmp_ne_u32 s[42:43], 0, v5\n"
    ".Lfk_s51:\n"
    "s_cmp_eq_u64 s[44:45], 0\n"
    "s_cbranch_scc1 .Lfk_s52\n"
    "v_xor_b32 v16, s48, v16\n"
    "v_xor_b32 v17, s48, v17\n"
    "v_xor_b32 v18, s48, v18\n"
    "v_xor_b32 v19, s48, v19\n"
    "v_or3_b32 v5, v16, v17, v18\n"
    "v_bitop3_b32 v5, v5, v19, 15 bitop3:0xa8\n"
    "v_cmp_ne_u32 s[44:45], 0, v5\n"
    ".Lfk_s52:\n"
    "s_cmp_eq_u64 s[46:47], 0\n"
    "s_cbranch_scc1 .Lfk_s53\n"
    "v_xor_b32 v20, s48, v20\n"
    "v_xor_b32 v21, s48, v21\n"
    "v_xor_b32 v22, s48, v22\n"
    "v_xor_b32 v23, s48, v23\n"
    "v_or3_b32 v5, v20, v21, v22\n"
    "v_bitop3_b32 v5, v5, v23, 15 bitop3:0xa8\n"
    "v_cmp_ne_u32 s[46:47], 0, v5\n"
    ".Lfk_s53:\n"
    "s_or_b64 vcc, s[40:41], s[42:43]\n"
    "s_or_b64 vcc, vcc, s[44:45]\n"
    "s_or_b64 vcc, vcc, s[46:47]\n"
    "s_cbranch_scc0 .Lfk_go45\n"
    "s_cmp_lt_u32 s62, 0x20000\n"
    "s_cbranch_scc1 .Lfk_g44\n"
    ".Lfk_go45:\n"
    "v_pk_add_f32 v[8:9], v[8:9], v[12:13]\n"
    "v_pk_add_f32 v[10:11], v[10:11], v[14:15]\n"
    "v_pk_add_f32 v[16:17], v[16:17], v[20:21]\n"
    "v_pk_add_f32 v[18:19], v[18:19], v[22:23]\n"
    "v_pk_add_f32 v[24:25], v[8:9], v[16:17]\n"
    "v_pk_add_f32 v[26:27], v[10:11], v[18:19]\n"
    "v_cvt_pk_bf16_f32 v6, v24, v25\n"
    "v_cvt_pk_bf16_f32 v7, v26, v27\n"
    "ds_write_b64 v3, v[6:7] offset:4096\n"
    "s_mov_b64 exec, 1\n"
    "v_mov_b32 v4, 1\n"
    "ds_add_u32 v0, v4 offset:12\n"
    "s_mov_b64 exec, -1\n"
    ".Lfk_tail:\n"
    "s_setprio 0\n"
    "s_mov_b32 s50, 0xfffffff0\n"
    "s_mov_b32 s51, 0x05040100\n"
    "s_mov_b32 s52, 0x07060302\n"
    "s_sub_u32 s31, s26, 14\n"
    "s_cmp_lt_u32 s26, 14\n"
    "s_cselect_b32 s31, s26, s31\n"
    "s_cmp_ge_u32 s31, 7\n"
    "s_cselect_b32 s30, 1, 0\n"
    "s_mul_i32 s53, s30, 7\n"
    "s_sub_u32 s31, s31, s53\n"
    "v_lshrrev_b32 v2, 4, v1\n"
    "v_and_b32 v3, 3, v1\n"
    "v_bfe_u32 v6, v1, 3, 1\n"
    "v_bfe_u32 v7, v1, 2, 1\n"
    "v_lshlrev_b32 v5, 6, v2\n"
    "v_lshl_add_u32 v5, v3, 4, v5\n"
    "v_lshl_add_u32 v5, v6, 10, v5\n"
    "s_cmp_ge_u32 s26, 14\n"
    "s_cbranch_scc1 .Lfk_ffn\n"
    "s_xor_b32 s3, s25, 4\n"
    "s_lshl3_add_u32 s53, s3, s24\n"
    "s_mul_i32 s54, s53, 57344\n"
    "s_lshl_b32 s55, s31, 7\n"
    "s_add_u32 s54, s54, s55\n"
    "v_lshrrev_b32 v148, 3, v1\n"
    "v_mul_u32_u24 v140, 7168, v148\n"
    "v_and_b32 v141, 7, v1\n"
    "v_lshl_add_u32 v140, v141, 4, v140\n"
    "s_movk_i32 s60, 3584\n"
    "s_add_u32 s36, s12, s54\n"
    "s_addc_u32 s37, s13, 0\n"
    "s_and_b32 s37, s37, 0xffff\n"
    "s_mov_b32 s39, 0x20000\n"
    "s_sub_u32 s38, 3670016, s54\n"
    "s_lshl_b32 s53, s53, 13\n"
    "s_lshl_b32 s54, s30, 12\n"
    "s_add_u32 s53, s53, s54\n"
    "s_add_u32 s53, s53, 327680\n"
    "s_mov_b32 s59, s53\n"
    "v_lshl_add_u32 v143, v7, 11, v5\n"
    "v_add_u32 v143, s53, v143\n"
    "s_mul_i32 s53, s24, 640\n"
    "v_lshlrev_b32 v144, 4, v1\n"
    "v_add_u32 v144, s53, v144\n"
    "s_and_b32 s53, s24, 3\n"
    "s_lshl_b32 s54, s53, 7\n"
    "v_mul_u32_u24 v145, 640, v148\n"
    "v_lshl_add_u32 v145, v141, 4, v145\n"
    "v_add_u32 v145, s54, v145\n"
    "s_lshr_b32 s54, s53, 1\n"
    "s_lshl1_add_u32 s54, s30, s54\n"
    "s_mul_i32 s54, s54, 7\n"
    "s_add_u32 s54, s54, s31\n"
    "s_lshl3_add_u32 s54, s54, s3\n"
    "s_lshl_b32 s54, s54, 8\n"
    "s_and_b32 s53, s53, 1\n"
    "s_lshl_b32 s53, s53, 7\n"
    "s_add_u32 s54, s54, s53\n"
    "s_lshr_b32 s53, s24, 2\n"
    "s_mul_i32 s55, s53, 229376\n"
    "s_add_u32 s54, s54, s55\n"
    "s_add_u32 s54, s54, 851968\n"
    "v_lshrrev_b32 v149, 5, v1\n"
    "v_mul_u32_u24 v146, 57344, v149\n"
    "v_and_b32 v149, 31, v1\n"
    "v_lshl_add_u32 v146, v149, 2, v146\n"
    "v_add_u32 v146, s54, v146\n"
    "s_mov_b32 s61, 114688\n"
    "s_lshl_b32 s53, s53, 6\n"
    "v_lshlrev_b32 v147, 5, v7\n"
    "v_lshl_add_u32 v147, v6, 4, v147\n"
    "v_add_u32 v147, s53, v147\n"
    "s_mov_b32 s63, 0x400000\n"
    ".Lfk_sp54:\n"
    "ds_read_b32 v150, v0 offset:24\n"
    "s_sub_u32 s63, s63, 1\n"
    "s_cbranch_scc1 .Lfk_so55\n"
    "s_waitcnt lgkmcnt(0)\n"
    "v_cmp_gt_u32 vcc, 4, v150\n"
    "s_cbranch_vccz .Lfk_so55\n"
    "s_sleep 2\n"
    "s_branch .Lfk_sp54\n"
    ".Lfk_so55:\n"
    "s_waitcnt lgkmcnt(0)\n"
    "buffer_load_dwordx4 v[40:43], v140, s[36:39], 0 offen offset:0\n"
    "buffer_load_dwordx4 v[44:47], v140, s[36:39], 0 offen offset:896\n"
    "s_mov_b32 s63, 0x400000\n"
    ".Lfk_sp56:\n"
    "ds_read_b32 v150, v0 offset:28\n"
    "s_sub_u32 s63, s63, 1\n"
    "s_cbranch_scc1 .Lfk_so57\n"
    "s_waitcnt lgkmcnt(0)\n"
    "v_cmp_gt_u32 vcc, 4, v150\n"
    "s_cbranch_vccz .Lfk_so57\n"
    "s_sleep 1\n"
    "s_branch .Lfk_sp56\n"
    ".Lfk_so57:\n"
    "s_waitcnt lgkmcnt(0)\n"
    "buffer_load_dwordx4 v[48:51], v140, s[36:39], 0 offen offset:1792\n"
    "buffer_load_dwordx4 v[52:55], v140, s[36:39], 0 offen offset:2688\n"
    "buffer_load_dwordx4 v[56:59], v140, s[36:39], s60 offen offset:0\n"
    "buffer_load_dwordx4 v[60:63], v140, s[36:39], s60 offen offset:896\n"
    "buffer_load_dwordx4 v[64:67], v140, s[36:39], s60 offen offset:1792\n"
    "buffer_load_dwordx4 v[68:71], v140, s[36:39], s60 offen offset:2688\n"
    "ds_read_b32 v150, v0 offset:64\n"
    "s_waitcnt lgkmcnt(0)\n"
    "v_readfirstlane_b32 s49, v150\n"
    "s_nop 0\n"
    "s_and_b32 s48, s49, 15\n"
    "v_mov_b32 v151, s48\n"
    "s_sleep 4\n"
    "buffer_load_dwordx4 v[72:75], v143, s[32:35], 0 offen offset:0 sc1\n"
    "buffer_load_dwordx4 v[76:79], v143, s[32:35], 0 offen offset:256 sc1\n"
    "buffer_load_dwordx4 v[80:83], v143, s[32:35], 0 offen offset:512 sc1\n"
    "buffer_load_dwordx4 v[84:87], v143, s[32:35], 0 offen offset:768 sc1\n"
    "s_mov_b32 s62, 0\n"
    "s_mov_b32 s63, 0x400000\n"
    ".Lfk_sp60:\n"
    "ds_read_b32 v150, v0 offset:32\n"
    "s_sub_u32 s63, s63, 1\n"
    "s_cbranch_scc1 .Lfk_so61\n"
    "s_waitcnt lgkmcnt(0)\n"
    "v_cmp_gt_u32 vcc, 1, v150\n"
    "s_cbranch_vccnz .Lfk_sp60\n"
    ".Lfk_so61:\n"
    "s_waitcnt lgkmcnt(0)\n"
    "ds_read_b128 v[88:91], v147 offset:192\n"
    "s_waitcnt vmcnt(10)\n"
    "v_perm_b32 v8, v44, v40, s51\n"
    "v_perm_b32 v12, v44, v40, s52\n"
    "v_perm_b32 v16, v45, v41, s51\n"
    "v_perm_b32 v20, v45, v41, s52\n"
    "v_perm_b32 v24, v46, v42, s51\n"
    "v_perm_b32 v28, v46, v42, s52\n"
    "v_perm_b32 v32, v47, v43, s51\n"
    "v_perm_b32 v36, v47, v43, s52\n"
    "s_waitcnt vmcnt(8)\n"
    "v_perm_b32 v9, v52, v48, s51\n"
    "v_perm_b32 v13, v52, v48, s52\n"
    "v_perm_b32 v17, v53, v49, s51\n"
    "v_perm_b32 v21, v53, v49, s52\n"
    "v_perm_b32 v25, v54, v50, s51\n"
    "v_perm_b32 v29, v54, v50, s52\n"
    "v_perm_b32 v33, v55, v51, s51\n"
    "v_perm_b32 v37, v55, v51, s52\n"
    "s_waitcnt vmcnt(6)\n"
    "v_perm_b32 v10, v60, v56, s51\n"
    "v_perm_b32 v14, v60, v56, s52\n"
    "v_perm_b32 v18, v61, v57, s51\n"
    "v_perm_b32 v22, v61, v57, s52\n"
    "v_perm_b32 v26, v62, v58, s51\n"
    "v_perm_b32 v30, v62, v58, s52\n"
    "v_perm_b32 v34, v63, v59, s51\n"
    "v_perm_b32 v38, v63, v59, s52\n"
    "s_waitcnt vmcnt(4)\n"
    "v_perm_b32 v11, v68, v64, s51\n"
    "v_perm_b32 v15, v68, v64, s52\n"
    "v_perm_b32 v19, v69, v65, s51\n"
    "v_perm_b32 v23, v69, v65, s52\n"
    "v_perm_b32 v27, v70, v66, s51\n"
    "v_perm_b32 v31, v70, v66, s52\n"
    "v_perm_b32 v35, v71, v67, s51\n"
    "v_perm_b32 v39, v71, v67, s52\n"
    "s_waitcnt lgkmcnt(0)\n"
    "s_waitcnt vmcnt(3)\n"
    "v_xor_b32 v72, s48, v72\n"
    "v_xor_b32 v73, s48, v73\n"
    "v_xor_b32 v74, s48, v74\n"
    "v_xor_b32 v75, s48, v75\n"
    "v_or3_b32 v96, v72, v73, v74\n"
    "v_bitop3_b32 v96, v96, v75, 15 bitop3:0xa8\n"
    "v_cmp_ne_u32 s[4:5], 0, v96\n"
    "s_waitcnt vmcnt(2)\n"
    "v_xor_b32 v76, s48, v76\n"
    "v_xor_b32 v77, s48, v77\n"
    "v_xor_b32 v78, s48, v78\n"
    "v_xor_b32 v79, s48, v79\n"
    "v_or3_b32 v96, v76, v77, v78\n"
    "v_bitop3_b32 v96, v96, v79, 15 bitop3:0xa8\n"
    "v_cmp_ne_u32 s[6:7], 0, v96\n"
    "s_waitcnt vmcnt(1)\n"
    "v_xor_b32 v80, s48, v80\n"
    "v_xor_b32 v81, s48, v81\n"
    "v_xor_b32 v82, s48, v82\n"
    "v_xor_b32 v83, s48, v83\n"
    "v_or3_b32 v96, v80, v81, v82\n"
    "v_bitop3_b32 v96, v96, v83, 15 bitop3:0xa8\n"
    "v_cmp_ne_u32 s[8:9], 0, v96\n"
    "s_waitcnt vmcnt(0)\n"
    "v_xor_b32 v84, s48, v84\n"
    "v_xor_b32 v85, s48, v85\n"
    "v_xor_b32 v86, s48, v86\n"
    "v_xor_b32 v87, s48, v87\n"
    "v_or3_b32 v96, v84, v85, v86\n"
    "v_bitop3_b32 v96, v96, v87, 15 bitop3:0xa8\n"
    "v_cmp_ne_u32 s[10:11], 0, v96\n"
    "s_or_b64 vcc, s[4:5], s[6:7]\n"
    "s_or_b64 vcc, vcc, s[8:9]\n"
    "s_or_b64 vcc, vcc, s[10:11]\n"
    "s_cbranch_scc0 .Lfk_go59\n"
    ".Lfk_g58:\n"
    "s_cmp_eq_u64 s[4:5], 0\n"
    "s_cbranch_scc1 .Lfk_s62\n"
    "buffer_load_dwordx4 v[72:75], v143, s[32:35], 0 offen offset:0 sc1\n"
    ".Lfk_s62:\n"
    "s_cmp_eq_u64 s[6:7], 0\n"
    "s_cbranch_scc1 .Lfk_s63\n"
    "buffer_load_dwordx4 v[76:79], v143, s[32:35], 0 offen offset:256 sc1\n"
    ".Lfk_s63:\n"
    "s_cmp_eq_u64 s[8:9], 0\n"
    "s_cbranch_scc1 .Lfk_s64\n"
    "buffer_load_dwordx4 v[80:83], v143, s[32:35], 0 offen offset:512 sc1\n"
    ".Lfk_s64:\n"
    "s_cmp_eq_u64 s[10:11], 0\n"
    "s_cbranch_scc1 .Lfk_s65\n"
    "buffer_load_dwordx4 v[84:87], v143, s[32:35], 0 offen offset:768 sc1\n"
    ".Lfk_s65:\n"
    "s_add_u32 s62, s62, 1\n"
    "s_waitcnt vmcnt(0)\n"
    "s_cmp_eq_u64 s[4:5], 0\n"
    "s_cbranch_scc1 .Lfk_s66\n"
    "v_xor_b32 v72, s48, v72\n"
    "v_xor_b32 v73, s48, v73\n"
    "v_xor_b32 v74, s48, v74\n"
    "v_xor_b32 v75, s48, v75\n"
    "v_or3_b32 v96, v72, v73, v74\n"
    "v_bitop3_b32 v96, v96, v75, 15 bitop3:0xa8\n"
    "v_cmp_ne_u32 s[4:5], 0, v96\n"
    ".Lfk_s66:\n"
    "s_cmp_eq_u64 s[6:7], 0\n"
    "s_cbranch_scc1 .Lfk_s67\n"
    "v_xor_b32 v76, s48, v76\n"
    "v_xor_b32 v77, s48, v77\n"
    "v_xor_b32 v78, s48, v78\n"
    "v_xor_b32 v79, s48, v79\n"
    "v_or3_b32 v96, v76, v77, v78\n"
    "v_bitop3_b32 v96, v96, v79, 15 bitop3:0xa8\n"
    "v_cmp_ne_u32 s[6:7], 0, v96\n"
    ".Lfk_s67:\n"
    "s_cmp_eq_u64 s[8:9], 0\n"
    "s_cbranch_scc1 .Lfk_s68\n"
    "v_xor_b32 v80, s48, v80\n"
    "v_xor_b32 v81, s48, v81\n"
    "v_xor_b32 v82, s48, v82\n"
    "v_xor_b32 v83, s48, v83\n"
    "v_or3_b32 v96, v80, v81, v82\n"
    "v_bitop3_b32 v96, v96, v83, 15 bitop3:0xa8\n"
    "v_cmp_ne_u32 s[8:9], 0, v96\n"
    ".Lfk_s68:\n"
    "s_cmp_eq_u64 s[10:11], 0\n"
    "s_cbranch_scc1 .Lfk_s69\n"
    "v_xor_b32 v84, s48, v84\n"
    "v_xor_b32 v85, s48, v85\n"
    "v_xor_b32 v86, s48, v86\n"
    "v_xor_b32 v87, s48, v87\n"
    "v_or3_b32 v96, v84, v85, v86\n"
    "v_bitop3_b32 v96, v96, v87, 15 bitop3:0xa8\n"
    "v_cmp_ne_u32 s[10:11], 0, v96\n"
    ".Lfk_s69:\n"
    "s_or_b64 vcc, s[4:5], s[6:7]\n"
    "s_or_b64 vcc, vcc, s[8:9]\n"
    "s_or_b64 vcc, vcc, s[10:11]\n"
    "s_cbranch_scc0 .Lfk_go59\n"
    "s_cmp_lt_u32 s62, 0x20000\n"
    "s_cbranch_scc1 .Lfk_g58\n"
    ".Lfk_go59:\n"
    "v_mul_f32 v92, v72, v88\n"
    "v_mul_f32 v93, v73, v88\n"
    "v_mul_f32 v94, v74, v88\n"
    "v_mul_f32 v95, v75, v88\n"
    "v_fmac_f32 v92, v76, v89\n"
    "v_fmac_f32 v93, v77, v89\n"
    "v_fmac_f32 v94, v78, v89\n"
    "v_fmac_f32 v95, v79, v89\n"
    "v_fmac_f32 v92, v80, v90\n"
    "v_fmac_f32 v93, v81, v90\n"
    "v_fmac_f32 v94, v82, v90\n"
    "v_fmac_f32 v95, v83, v90\n"
    "v_fmac_f32 v92, v84, v91\n"
    "v_fmac_f32 v93, v85, v91\n"
    "v_fmac_f32 v94, v86, v91\n"
    "v_fmac_f32 v95, v87, v91\n"
    "v_add_f32_dpp v92, v92, v92 row_ror:8 row_mask:0xf bank_mask:0xf\n"
    "v_add_f32_dpp v93, v93, v93 row_ror:8 row_mask:0xf bank_mask:0xf\n"
    "v_add_f32_dpp v94, v94, v94 row_ror:8 row_mask:0xf bank_mask:0xf\n"
    "v_add_f32_dpp v95, v95, v95 row_ror:8 row_mask:0xf bank_mask:0xf\n"
    "v_cvt_pk_bf16_f32 v97, v92, v93\n"
    "v_cvt_pk_bf16_f32 v98, v94, v95\n"
    "s_nop 0\n"
    "v_mov_b32_dpp v100, v97 quad_perm:[0,0,2,2] row_mask:0xf bank_mask:0xf\n"
    "v_mov_b32_dpp v102, v97 quad_perm:[1,1,3,3] row_mask:0xf bank_mask:0xf\n"
    "v_mov_b32_dpp v101, v98 quad_perm:[0,0,2,2] row_mask:0xf bank_mask:0xf\n"
    "v_mov_b32_dpp v103, v98 quad_perm:[1,1,3,3] row_mask:0xf bank_mask:0xf\n"
    "s_nop 1\n"
    "v_mfma_f32_16x16x32_bf16 v[104:107], v[100:103], v[8:11], 0\n"
    "v_mfma_f32_16x16x32_bf16 v[108:111], v[100:103], v[12:15], 0\n"
    "v_mfma_f32_16x16x32_bf16 v[112:115], v[100:103], v[16:19], 0\n"
    "v_mfma_f32_16x16x32_bf16 v[116:119], v[100:103], v[20:23], 0\n"
    "v_mfma_f32_16x16x32_bf16 v[120:123], v[100:103], v[24:27], 0\n"
    "v_mfma_f32_16x16x32_bf16 v[124:127], v[100:103], v[28:31], 0\n"
    "v_mfma_f32_16x16x32_bf16 v[128:131], v[100:103], v[32:35], 0\n"
    "v_mfma_f32_16x16x32_bf16 v[132:135], v[100:103], v[36:39], 0\n"
    "s_nop 0\n"
    "v_add_f32_dpp v72, v106, v104 row_ror:8 row_mask:0xf bank_mask:0x3\n"
    "v_add_f32_dpp v73, v110, v108 row_ror:8 row_mask:0xf bank_mask:0x3\n"
    "v_add_f32_dpp v74, v114, v112 row_ror:8 row_mask:0xf bank_mask:0x3\n"
    "v_add_f32_dpp v75, v118, v116 row_ror:8 row_mask:0xf bank_mask:0x3\n"
    "v_add_f32_dpp v72, v120, v122 row_ror:8 row_mask:0xf bank_mask:0xc\n"
    "v_add_f32_dpp v73, v124, v126 row_ror:8 row_mask:0xf bank_mask:0xc\n"
    "v_add_f32_dpp v74, v128, v130 row_ror:8 row_mask:0xf bank_mask:0xc\n"
    "v_add_f32_dpp v75, v132, v134 row_ror:8 row_mask:0xf bank_mask:0xc\n"
    "s_mov_b32 exec_hi, 0\n"
    "ds_write_b128 v144, v[72:75] offset:7680\n"
    "s_mov_b32 exec_hi, -1\n"
    "s_waitcnt lgkmcnt(0)\n"
    "s_barrier\n"
    "ds_read_b128 v[76:79], v145 offset:7680\n"
    "s_waitcnt lgkmcnt(0)\n"
    "v_add_f32_dpp v80, v76, v76 row_ror:8 row_mask:0xf bank_mask:0x3\n"
    "v_add_f32_dpp v81, v78, v78 row_ror:8 row_mask:0xf bank_mask:0x3\n"
    "s_nop 0\n"
    "v_add_f32_dpp v80, v77, v77 row_ror:8 row_mask:0xf bank_mask:0xc\n"
    "v_add_f32_dpp v81, v79, v79 row_ror:8 row_mask:0xf bank_mask:0xc\n"
    "s_nop 1\n"
    "v_permlane16_swap_b32 v80, v81\n"
    "v_add_f32 v80, v80, v81\n"
    "v_mov_b32 v81, v80\n"
    "s_nop 1\n"
    "v_permlane32_swap_b32 v80, v81\n"
    "v_add_f32 v80, v80, v81\n"
    "v_and_or_b32 v80, v80, s50, v151\n"
    "buffer_store_dword v80, v146, s[32:35], 0 offen sc1\n"
    "buffer_store_dword v80, v146, s[32:35], s61 offen sc1\n"
    "s_endpgm\n"
    ".Lfk_ffn:\n"
    "s_cmp_ge_u32 s26, 28\n"
    "s_cbranch_scc1 .Lfk_done\n"
    "s_mul_i32 s54, s25, 200704\n"
    "s_add_u32 s44, s18, s54\n"
    "s_addc_u32 s45, s19, 0\n"
    "s_and_b32 s45, s45, 0xffff\n"
    "s_mov_b32 s46, 200704\n"
    "s_mov_b32 s47, 0x20000\n"
    "s_mul_i32 s29, s25, 7\n"
    "s_add_u32 s29, s29, s31\n"
    "v_lshrrev_b32 v12, 3, v1\n"
    "v_mul_u32_u24 v12, 3584, v12\n"
    "v_lshl_add_u32 v12, v3, 4, v12\n"
    "s_mul_i32 s53, s24, 229376\n"
    "s_lshl_b32 s54, s29, 6\n"
    "s_add_u32 s53, s53, s54\n"
    "v_add_u32 v12, s53, v12\n"
    "v_mov_b32 v13, 0\n"
    "s_mov_b32 s56, 0xf0f0f0f0\n"
    "s_mov_b32 s57, 0xf0f0f0f0\n"
    "v_mov_b32 v14, s14\n"
    "v_mov_b32 v15, s16\n"
    "v_cndmask_b32 v160, v14, v15, s[56:57]\n"
    "v_mov_b32 v14, s15\n"
    "v_mov_b32 v15, s17\n"
    "v_cndmask_b32 v161, v14, v15, s[56:57]\n"
    "v_lshl_add_u64 v[160:161], v[12:13], 0, v[160:161]\n"
    "v_mul_u32_u24 v162, 7168, v2\n"
    "v_and_b32 v14, 15, v1\n"
    "v_lshl_add_u32 v162, v14, 3, v162\n"
    "s_mul_i32 s53, s31, 28672\n"
    "s_lshl_b32 s54, s24, 7\n"
    "s_add_u32 s53, s53, s54\n"
    "v_add_u32 v162, s53, v162\n"
    "v_mul_u32_u24 v156, 896, v7\n"
    "v_and_b32 v15, 1, v1\n"
    "v_lshl_add_u32 v156, v15, 6, v156\n"
    "v_lshl_add_u32 v156, v6, 5, v156\n"
    "v_lshl_add_u32 v156, v2, 2, v156\n"
    "v_and_b32 v15, 2, v1\n"
    "v_add3_u32 v156, v156, v15, s54\n"
    "s_mul_i32 s53, s30, 1792\n"
    "v_add_u32 v163, s53, v156\n"
    "s_mul_i32 s53, s30, 14\n"
    "s_add_u32 s53, s53, s24\n"
    "s_lshl_b32 s53, s53, 11\n"
    "s_lshr_b32 s55, s25, 0\n"
    "s_mul_i32 s55, s55, 57344\n"
    "s_add_u32 s53, s53, s55\n"
    "s_add_u32 s53, s53, 851968\n"
    "v_mul_u32_u24 v164, 14336, v7\n"
    "v_add3_u32 v164, v164, v5, s53\n"
    "v_mul_u32_u24 v165, 320, v2\n"
    "v_lshl_add_u32 v165, v7, 7, v165\n"
    "v_lshl_add_u32 v165, v3, 5, v165\n"
    "v_lshl_add_u32 v165, v6, 4, v165\n"
    "s_lshl_b32 s53, s24, 10\n"
    "v_add_u32 v165, s53, v165\n"
    "v_lshlrev_b32 v4, 7, v6\n"
    "v_lshl_add_u32 v4, v2, 5, v4\n"
    "v_lshl_add_u32 v4, v3, 3, v4\n"
    "v_mul_u32_u24 v166, 320, v7\n"
    "v_add_u32 v166, v166, v4\n"
    "v_lshlrev_b32 v157, 5, v7\n"
    "s_lshl_b32 s53, s24, 2\n"
    "s_add_u32 s53, s53, 5632\n"
    "v_add_u32 v158, s53, v157\n"
    "s_mul_i32 s53, s30, 112\n"
    "s_add_u32 s53, s53, s29\n"
    "s_mul_i32 s53, s53, 1792\n"
    "s_lshl_b32 s54, s24, 8\n"
    "s_add_u32 s53, s53, s54\n"
    "s_add_u32 s53, s53, 1310720\n"
    "v_mul_u32_u24 v159, 100352, v2\n"
    "v_lshl_add_u32 v159, v14, 4, v159\n"
    "v_add_u32 v159, s53, v159\n"
    "s_movk_i32 s60, 28672\n"
    "s_mov_b32 s61, 0\n"
    "s_movk_i32 s59, 3584\n"
    "s_mov_b32 s36, s4\n"
    "s_and_b32 s37, s5, 0xffff\n"
    "s_mov_b32 s38, 3584\n"
    "s_mov_b32 s39, 0x20000\n"
    "s_cmp_eq_u32 s24, 7\n"
    "s_cbranch_scc1 .Lfk_fin\n"
    "s_mov_b32 s63, 0x400000\n"
    ".Lfk_sp70:\n"
    "ds_read_b32 v10, v0 offset:36\n"
    "s_sub_u32 s63, s63, 1\n"
    "s_cbranch_scc1 .Lfk_so71\n"
    "s_waitcnt lgkmcnt(0)\n"
    "v_cmp_gt_u32 vcc, 4, v10\n"
    "s_cbranch_vccz .Lfk_so71\n"
    "s_sleep 2\n"
    "s_branch .Lfk_sp70\n"
    ".Lfk_so71:\n"
    "s_waitcnt lgkmcnt(0)\n"
    "v_mov_b32 v2, 0\n"
    "v_mov_b32 v3, 0\n"
    "buffer_load_short_d16_hi v2, v163, s[36:39], 0 offen\n"
    "buffer_load_short_d16_hi v3, v163, s[36:39], 0 offen offset:16\n"
    "global_load_dwordx4 v[40:43], v[160:161], off\n"
    "v_lshl_add_u64 v[160:161], v[160:161], 0, s[60:61]\n"
    "global_load_dwordx4 v[44:47], v[160:161], off\n"
    "v_lshl_add_u64 v[160:161], v[160:161], 0, s[60:61]\n"
    "global_load_dwordx4 v[48:51], v[160:161], off\n"
    "v_lshl_add_u64 v[160:161], v[160:161], 0, s[60:61]\n"
    "global_load_dwordx4 v[52:55], v[160:161], off\n"
    "v_lshl_add_u64 v[160:161], v[160:161], 0, s[60:61]\n"
    "global_load_dwordx4 v[56:59], v[160:161], off\n"
    "v_lshl_add_u64 v[160:161], v[160:161], 0, s[60:61]\n"
    "global_load_dwordx4 v[60:63], v[160:161], off\n"
    "v_lshl_add_u64 v[160:161], v[160:161], 0, s[60:61]\n"
    "global_load_dwordx4 v[64:67], v[160:161], off\n"
    "v_lshl_add_u64 v[160:161], v[160:161], 0, s[60:61]\n"
    "global_load_dwordx4 v[68:71], v[160:161], off\n"
    "buffer_load_dwordx2 v[72:73], v162, s[44:47], 0 offen offset:0\n"
    "buffer_load_dwordx2 v[74:75], v162, s[44:47], 0 offen offset:896\n"
    "buffer_load_dwordx2 v[76:77], v162, s[44:47], 0 offen offset:1792\n"
    "buffer_load_dwordx2 v[78:79], v162, s[44:47], 0 offen offset:2688\n"
    "buffer_load_dwordx2 v[80:81], v162, s[44:47], s59 offen offset:0\n"
    "buffer_load_dwordx2 v[82:83], v162, s[44:47], s59 offen offset:896\n"
    "buffer_load_dwordx2 v[84:85], v162, s[44:47], s59 offen offset:1792\n"
    "buffer_load_dwordx2 v[86:87], v162, s[44:47], s59 offen offset:2688\n"
    "ds_read_b32 v12, v0 offset:64\n"
    "s_waitcnt lgkmcnt(0)\n"
    "v_readfirstlane_b32 s49, v12\n"
    "s_nop 0\n"
    "s_and_b32 s48, s49, 15\n"
    "v_mov_b32 v167, s48\n"
    "v_mov_b32 v152, 0x3f803f80\n"
    "v_mov_b32 v153, 0x3f803f80\n"
    "v_mov_b32 v154, 0x3f803f80\n"
    "v_mov_b32 v155, 0x3f803f80\n"
    "s_waitcnt vmcnt(14)\n"
    "v_perm_b32 v8, v44, v40, s51\n"
    "v_perm_b32 v12, v44, v40, s52\n"
    "v_perm_b32 v16, v45, v41, s51\n"
    "v_perm_b32 v20, v45, v41, s52\n"
    "v_perm_b32 v24, v46, v42, s51\n"
    "v_perm_b32 v28, v46, v42, s52\n"
    "v_perm_b32 v32, v47, v43, s51\n"
    "v_perm_b32 v36, v47, v43, s52\n"
    "s_waitcnt vmcnt(12)\n"
    "v_perm_b32 v9, v52, v48, s51\n"
    "v_perm_b32 v13, v52, v48, s52\n"
    "v_perm_b32 v17, v53, v49, s51\n"
    "v_perm_b32 v21, v53, v49, s52\n"
    "v_perm_b32 v25, v54, v50, s51\n"
    "v_perm_b32 v29, v54, v50, s52\n"
    "v_perm_b32 v33, v55, v51, s51\n"
    "v_perm_b32 v37, v55, v51, s52\n"
    "s_waitcnt vmcnt(10)\n"
    "v_perm_b32 v10, v60, v56, s51\n"
    "v_perm_b32 v14, v60, v56, s52\n"
    "v_perm_b32 v18, v61, v57, s51\n"
    "v_perm_b32 v22, v61, v57, s52\n"
    "v_perm_b32 v26, v62, v58, s51\n"
    "v_perm_b32 v30, v62, v58, s52\n"
    "v_perm_b32 v34, v63, v59, s51\n"
    "v_perm_b32 v38, v63, v59, s52\n"
    "s_waitcnt vmcnt(8)\n"
    "v_perm_b32 v11, v68, v64, s51\n"
    "v_perm_b32 v15, v68, v64, s52\n"
    "v_perm_b32 v19, v69, v65, s51\n"
    "v_perm_b32 v23, v69, v65, s52\n"
    "v_perm_b32 v27, v70, v66, s51\n"
    "v_perm_b32 v31, v70, v66, s52\n"
    "v_perm_b32 v35, v71, v67, s51\n"
    "v_perm_b32 v39, v71, v67, s52\n"
    "s_waitcnt vmcnt(6)\n"
    "v_perm_b32 v88, v74, v72, s51\n"
    "v_perm_b32 v92, v74, v72, s52\n"
    "v_perm_b32 v96, v75, v73, s51\n"
    "v_perm_b32 v100, v75, v73, s52\n"
    "s_waitcnt vmcnt(4)\n"
    "v_perm_b32 v89, v78, v76, s51\n"
    "v_perm_b32 v93, v78, v76, s52\n"
    "v_perm_b32 v97, v79, v77, s51\n"
    "v_perm_b32 v101, v79, v77, s52\n"
    "s_waitcnt vmcnt(2)\n"
    "v_perm_b32 v90, v82, v80, s51\n"
    "v_perm_b32 v94, v82, v80, s52\n"
    "v_perm_b32 v98, v83, v81, s51\n"
    "v_perm_b32 v102, v83, v81, s52\n"
    "s_waitcnt vmcnt(0)\n"
    "v_perm_b32 v91, v86, v84, s51\n"
    "v_perm_b32 v95, v86, v84, s52\n"
    "v_perm_b32 v99, v87, v85, s51\n"
    "v_perm_b32 v103, v87, v85, s52\n"
    "s_barrier\n"
    "buffer_load_dwordx4 v[104:107], v164, s[32:35], 0 offen offset:0 sc1\n"
    "buffer_load_dwordx4 v[108:111], v164, s[32:35], 0 offen offset:256 sc1\n"
    "buffer_load_dwordx4 v[112:115], v164, s[32:35], 0 offen offset:512 sc1\n"
    "buffer_load_dwordx4 v[116:119], v164, s[32:35], 0 offen offset:768 sc1\n"
    "s_mov_b32 s62, 0\n"
    "s_waitcnt vmcnt(3)\n"
    "v_xor_b32 v104, s48, v104\n"
    "v_xor_b32 v105, s48, v105\n"
    "v_xor_b32 v106, s48, v106\n"
    "v_xor_b32 v107, s48, v107\n"
    "v_or3_b32 v5, v104, v105, v106\n"
    "v_bitop3_b32 v5, v5, v107, 15 bitop3:0xa8\n"
    "v_cmp_ne_u32 s[4:5], 0, v5\n"
    "s_waitcnt vmcnt(2)\n"
    "v_xor_b32 v108, s48, v108\n"
    "v_xor_b32 v109, s48, v109\n"
    "v_xor_b32 v110, s48, v110\n"
    "v_xor_b32 v111, s48, v111\n"
    "v_or3_b32 v5, v108, v109, v110\n"
    "v_bitop3_b32 v5, v5, v111, 15 bitop3:0xa8\n"
    "v_cmp_ne_u32 s[6:7], 0, v5\n"
    "s_waitcnt vmcnt(1)\n"
    "v_xor_b32 v112, s48, v112\n"
    "v_xor_b32 v113, s48, v113\n"
    "v_xor_b32 v114, s48, v114\n"
    "v_xor_b32 v115, s48, v115\n"
    "v_or3_b32 v5, v112, v113, v114\n"
    "v_bitop3_b32 v5, v5, v115, 15 bitop3:0xa8\n"
    "v_cmp_ne_u32 s[8:9], 0, v5\n"
    "s_waitcnt vmcnt(0)\n"
    "v_xor_b32 v116, s48, v116\n"
    "v_xor_b32 v117, s48, v117\n"
    "v_xor_b32 v118, s48, v118\n"
    "v_xor_b32 v119, s48, v119\n"
    "v_or3_b32 v5, v116, v117, v118\n"
    "v_bitop3_b32 v5, v5, v119, 15 bitop3:0xa8\n"
    "v_cmp_ne_u32 s[10:11], 0, v5\n"
    "s_or_b64 vcc, s[4:5], s[6:7]\n"
    "s_or_b64 vcc, vcc, s[8:9]\n"
    "s_or_b64 vcc, vcc, s[10:11]\n"
    "s_cbranch_scc0 .Lfk_go73\n"
    ".Lfk_g72:\n"
    "s_cmp_eq_u64 s[4:5], 0\n"
    "s_cbranch_scc1 .Lfk_s74\n"
    "buffer_load_dwordx4 v[104:107], v164, s[32:35], 0 offen offset:0 sc1\n"
    ".Lfk_s74:\n"
    "s_cmp_eq_u64 s[6:7], 0\n"
    "s_cbranch_scc1 .Lfk_s75\n"
    "buffer_load_dwordx4 v[108:111], v164, s[32:35], 0 offen offset:256 sc1\n"
    ".Lfk_s75:\n"
    "s_cmp_eq_u64 s[8:9], 0\n"
    "s_cbranch_scc1 .Lfk_s76\n"
    "buffer_load_dwordx4 v[112:115], v164, s[32:35], 0 offen offset:512 sc1\n"
    ".Lfk_s76:\n"
    "s_cmp_eq_u64 s[10:11], 0\n"
    "s_cbranch_scc1 .Lfk_s77\n"
    "buffer_load_dwordx4 v[116:119], v164, s[32:35], 0 offen offset:768 sc1\n"
    ".Lfk_s77:\n"
    "s_add_u32 s62, s62, 1\n"
    "s_waitcnt vmcnt(0)\n"
    "s_cmp_eq_u64 s[4:5], 0\n"
    "s_cbranch_scc1 .Lfk_s78\n"
    "v_xor_b32 v104, s48, v104\n"
    "v_xor_b32 v105, s48, v105\n"
    "v_xor_b32 v106, s48, v106\n"
    "v_xor_b32 v107, s48, v107\n"
    "v_or3_b32 v5, v104, v105, v106\n"
    "v_bitop3_b32 v5, v5, v107, 15 bitop3:0xa8\n"
    "v_cmp_ne_u32 s[4:5], 0, v5\n"
    ".Lfk_s78:\n"
    "s_cmp_eq_u64 s[6:7], 0\n"
    "s_cbranch_scc1 .Lfk_s79\n"
    "v_xor_b32 v108, s48, v108\n"
    "v_xor_b32 v109, s48, v109\n"
    "v_xor_b32 v110, s48, v110\n"
    "v_xor_b32 v111, s48, v111\n"
    "v_or3_b32 v5, v108, v109, v110\n"
    "v_bitop3_b32 v5, v5, v111, 15 bitop3:0xa8\n"
    "v_cmp_ne_u32 s[6:7], 0, v5\n"
    ".Lfk_s79:\n"
    "s_cmp_eq_u64 s[8:9], 0\n"
    "s_cbranch_scc1 .Lfk_s80\n"
    "v_xor_b32 v112, s48, v112\n"
    "v_xor_b32 v113, s48, v113\n"
    "v_xor_b32 v114, s48, v114\n"
    "v_xor_b32 v115, s48, v115\n"
    "v_or3_b32 v5, v112, v113, v114\n"
    "v_bitop3_b32 v5, v5, v115, 15 bitop3:0xa8\n"
    "v_cmp_ne_u32 s[8:9], 0, v5\n"
    ".Lfk_s80:\n"
    "s_cmp_eq_u64 s[10:11], 0\n"
    "s_cbranch_scc1 .Lfk_s81\n"
    "v_xor_b32 v116, s48, v116\n"
    "v_xor_b32 v117, s48, v117\n"
    "v_xor_b32 v118, s48, v118\n"
    "v_xor_b32 v119, s48, v119\n"
    "v_or3_b32 v5, v116, v117, v118\n"
    "v_bitop3_b32 v5, v5, v119, 15 bitop3:0xa8\n"
    "v_cmp_ne_u32 s[10:11], 0, v5\n"
    ".Lfk_s81:\n"
    "s_or_b64 vcc, s[4:5], s[6:7]\n"
    "s_or_b64 vcc, vcc, s[8:9]\n"
    "s_or_b64 vcc, vcc, s[10:11]\n"
    "s_cbranch_scc0 .Lfk_go73\n"
    "s_cmp_lt_u32 s62, 0x20000\n"
    "s_cbranch_scc1 .Lfk_g72\n"
    ".Lfk_go73:\n"
    "v_pk_add_f32 v[104:105], v[104:105], v[108:109]\n"
    "v_pk_add_f32 v[106:107], v[106:107], v[110:111]\n"
    "v_pk_add_f32 v[112:113], v[112:113], v[116:117]\n"
    "v_pk_add_f32 v[114:115], v[114:115], v[118:119]\n"
    "v_pk_add_f32 v[72:73], v[104:105], v[112:113]\n"
    "v_pk_add_f32 v[74:75], v[106:107], v[114:115]\n"
    "s_nop 0\n"
    "v_add_f32_dpp v76, v72, v72 row_ror:8 row_mask:0xf bank_mask:0x3\n"
    "v_add_f32_dpp v77, v73, v73 row_ror:8 row_mask:0xf bank_mask:0x3\n"
    "s_nop 0\n"
    "v_add_f32_dpp v76, v74, v74 row_ror:8 row_mask:0xf bank_mask:0xc\n"
    "v_add_f32_dpp v77, v75, v75 row_ror:8 row_mask:0xf bank_mask:0xc\n"
    "v_cvt_pk_bf16_f32 v74, v76, v77\n"
    "v_lshlrev_b32 v72, 16, v74\n"
    "v_and_b32 v73, 0xffff0000, v74\n"
    "v_pk_add_f32 v[72:73], v[72:73], v[2:3]\n"
    "v_cvt_pk_bf16_f32 v76, v72, v73\n"
    "v_lshlrev_b32 v72, 16, v76\n"
    "v_and_b32 v73, 0xffff0000, v76\n"
    "v_mov_b32_dpp v77, v76 row_ror:8 row_mask:0xf bank_mask:0xf\n"
    "v_mov_b32_dpp v152, v76 quad_perm:[0,0,2,2] row_mask:0xf bank_mask:0x3\n"
    "v_mov_b32_dpp v154, v76 quad_perm:[1,1,3,3] row_mask:0xf bank_mask:0x3\n"
    "v_mov_b32_dpp v153, v77 quad_perm:[0,0,2,2] row_mask:0xf bank_mask:0x3\n"
    "v_mov_b32_dpp v155, v77 quad_perm:[1,1,3,3] row_mask:0xf bank_mask:0x3\n"
    "v_add_f32 v74, v72, v73\n"
    "ds_write_b16 v156, v76 offset:5888\n"
    "v_mfma_f32_16x16x32_bf16 v[120:123], v[152:155], v[8:11], 0\n"
    "ds_write_b16_d16_hi v156, v76 offset:5904\n"
    "v_add_f32_dpp v74, v74, v74 quad_perm:[1,0,3,2] row_mask:0xf bank_mask:0xf\n"
    "v_mfma_f32_16x16x32_bf16 v[124:127], v[152:155], v[12:15], 0\n"
    "s_nop 0\n"
    "v_add_f32_dpp v74, v74, v74 quad_perm:[2,3,0,1] row_mask:0xf bank_mask:0xf\n"
    "s_nop 1\n"
    "v_add_f32_dpp v74, v74, v74 row_ror:8 row_mask:0xf bank_mask:0xf\n"
    "v_mfma_f32_16x16x32_bf16 v[128:131], v[152:155], v[16:19], 0\n"
    "v_mov_b32 v75, v74\n"
    "s_nop 1\n"
    "v_permlane16_swap_b32 v74, v75\n"
    "v_mfma_f32_16x16x32_bf16 v[132:135], v[152:155], v[20:23], 0\n"
    "v_add_f32 v74, v74, v75\n"
    "v_mov_b32 v75, v74\n"
    "v_mfma_f32_16x16x32_bf16 v[136:139], v[152:155], v[24:27], 0\n"
    "s_nop 0\n"
    "v_permlane32_swap_b32 v74, v75\n"
    "v_add_f32 v74, v74, v75\n"
    "v_mfma_f32_16x16x32_bf16 v[140:143], v[152:155], v[28:31], 0\n"
    "v_mul_f32 v74, 0x3c800000, v74\n"
    "v_sub_f32 v72, v72, v74\n"
    "v_mfma_f32_16x16x32_bf16 v[144:147], v[152:155], v[32:35], 0\n"
    "v_sub_f32 v73, v73, v74\n"
    "v_mul_f32 v72, v72, v72\n"
    "v_mfma_f32_16x16x32_bf16 v[148:151], v[152:155], v[36:39], 0\n"
    "v_fmac_f32 v72, v73, v73\n"
    "s_nop 1\n"
    "v_add_f32_dpp v72, v72, v72 quad_perm:[1,0,3,2] row_mask:0xf bank_mask:0xf\n"
    "v_add_f32_dpp v80, v122, v120 row_ror:8 row_mask:0xf bank_mask:0x3\n"
    "s_nop 0\n"
    "v_add_f32_dpp v72, v72, v72 quad_perm:[2,3,0,1] row_mask:0xf bank_mask:0xf\n"
    "v_add_f32_dpp v81, v126, v124 row_ror:8 row_mask:0xf bank_mask:0x3\n"
    "s_nop 0\n"
    "v_add_f32_dpp v72, v72, v72 row_ror:8 row_mask:0xf bank_mask:0xf\n"
    "v_add_f32_dpp v82, v130, v128 row_ror:8 row_mask:0xf bank_mask:0x3\n"
    "v_mov_b32 v75, v72\n"
    "v_add_f32_dpp v83, v134, v132 row_ror:8 row_mask:0xf bank_mask:0x3\n"
    "s_nop 0\n"
    "v_permlane16_swap_b32 v72, v75\n"
    "v_add_f32_dpp v80, v136, v138 row_ror:8 row_mask:0xf bank_mask:0xc\n"
    "v_add_f32 v72, v72, v75\n"
    "v_add_f32_dpp v81, v140, v142 row_ror:8 row_mask:0xf bank_mask:0xc\n"
    "v_mov_b32 v75, v72\n"
    "v_add_f32_dpp v82, v144, v146 row_ror:8 row_mask:0xf bank_mask:0xc\n"
    "s_nop 0\n"
    "v_permlane32_swap_b32 v72, v75\n"
    "v_add_f32_dpp v83, v148, v150 row_ror:8 row_mask:0xf bank_mask:0xc\n"
    "v_add_f32 v72, v72, v75\n"
    "ds_write2_b32 v158, v74, v72 offset0:0 offset1:16\n"
    "s_mov_b32 exec_hi, 0xffff\n"
    "ds_write_b128 v165, v[80:83] offset:7680\n"
    "s_mov_b32 exec_hi, -1\n"
    "s_waitcnt lgkmcnt(0)\n"
    "s_barrier\n"
    "ds_read_b128 v[104:107], v157 offset:5632\n"
    "ds_read_b128 v[112:115], v157 offset:5696\n"
    "ds_read_b128 v[108:111], v157 offset:5648\n"
    "ds_read_b128 v[116:119], v157 offset:5712\n"
    "ds_read_b64 v[40:41], v166 offset:7680\n"
    "ds_read_b64 v[42:43], v166 offset:8704\n"
    "ds_read_b64 v[44:45], v166 offset:9728\n"
    "ds_read_b64 v[46:47], v166 offset:10752\n"
    "ds_read_b64 v[48:49], v166 offset:11776\n"
    "ds_read_b64 v[50:51], v166 offset:12800\n"
    "ds_read_b64 v[52:53], v166 offset:13824\n"
    "s_waitcnt lgkmcnt(7)\n"
    "v_pk_add_f32 v[72:73], v[104:105], v[106:107]\n"
    "v_pk_add_f32 v[74:75], v[112:113], v[114:115]\n"
    "ds_read_b64 v[56:57], v4 offset:8320\n"
    "ds_read_b64 v[58:59], v4 offset:9344\n"
    "ds_read_b64 v[60:61], v4 offset:10368\n"
    "ds_read_b64 v[62:63], v4 offset:11392\n"
    "ds_read_b64 v[64:65], v4 offset:12416\n"
    "ds_read_b64 v[66:67], v4 offset:13440\n"
    "ds_read_b64 v[68:69], v4 offset:14464\n"
    "v_pk_add_f32 v[72:73], v[72:73], v[108:109]\n"
    "v_pk_add_f32 v[74:75], v[74:75], v[116:117]\n"
    "v_add_f32 v72, v72, v110\n"
    "v_add_f32 v74, v74, v118\n"
    "v_add_f32 v72, v72, v73\n"
    "v_add_f32 v74, v74, v75\n"
    "v_mul_f32 v72, 0x3e124925, v72\n"
    "v_pk_add_f32 v[104:105], v[104:105], v[72:73] op_sel_hi:[1,0] neg_lo:[0,1] neg_hi:[0,1]\n"
    "v_pk_add_f32 v[106:107], v[106:107], v[72:73] op_sel_hi:[1,0] neg_lo:[0,1] neg_hi:[0,1]\n"
    "v_pk_add_f32 v[108:109], v[108:109], v[72:73] op_sel_hi:[1,0] neg_lo:[0,1] neg_hi:[0,1]\n"
    "v_sub_f32 v110, v110, v72\n"
    "v_pk_mul_f32 v[104:105], v[104:105], v[104:105]\n"
    "v_cvt_pk_bf16_f32 v72, v72, v72\n"
    "v_pk_fma_f32 v[104:105], v[106:107], v[106:107], v[104:105]\n"
    "v_lshlrev_b32 v72, 16, v72\n"
    "v_pk_fma_f32 v[104:105], v[108:109], v[108:109], v[104:105]\n"
    "s_waitcnt lgkmcnt(7)\n"
    "v_pk_add_f32 v[40:41], v[40:41], v[42:43]\n"
    "v_fmac_f32 v104, v110, v110\n"
    "v_pk_add_f32 v[44:45], v[44:45], v[46:47]\n"
    "v_add_f32 v104, v104, v105\n"
    "v_pk_add_f32 v[48:49], v[48:49], v[50:51]\n"
    "v_fmac_f32 v74, 0x42800000, v104\n"
    "v_pk_add_f32 v[40:41], v[40:41], v[44:45]\n"
    "v_mul_f32 v74, 0x3b124925, v74\n"
    "v_pk_add_f32 v[48:49], v[48:49], v[52:53]\n"
    "v_cvt_pk_bf16_f32 v74, v74, v74\n"
    "v_pk_add_f32 v[40:41], v[40:41], v[48:49]\n"
    "v_lshlrev_b32 v74, 16, v74\n"
    "s_waitcnt lgkmcnt(0)\n"
    "v_pk_add_f32 v[56:57], v[56:57], v[58:59]\n"
    "v_add_f32 v74, 0x3727c5ac, v74\n"
    "v_pk_add_f32 v[60:61], v[60:61], v[62:63]\n"
    "v_cvt_pk_bf16_f32 v74, v74, v74\n"
    "v_pk_add_f32 v[64:65], v[64:65], v[66:67]\n"
    "v_lshlrev_b32 v74, 16, v74\n"
    "v_pk_add_f32 v[56:57], v[56:57], v[60:61]\n"
    "v_sqrt_f32 v74, v74\n"
    "v_pk_add_f32 v[64:65], v[64:65], v[68:69]\n"
    "v_cvt_pk_bf16_f32 v74, v74, v74\n"
    "v_pk_add_f32 v[56:57], v[56:57], v[64:65]\n"
    "v_lshlrev_b32 v74, 16, v74\n"
    "v_rcp_f32 v74, v74\n"
    "v_pk_fma_f32 v[40:41], v[56:57], v[72:73], v[40:41] op_sel_hi:[1,0,1] neg_lo:[1,0,0] neg_hi:[1,0,0]\n"
    "v_pk_mul_f32 v[40:41], v[40:41], v[74:75] op_sel_hi:[1,0]\n"
    "v_cvt_pk_bf16_f32 v44, v40, v41\n"
    "v_lshlrev_b32 v40, 16, v44\n"
    "v_and_b32 v41, 0xffff0000, v44\n"
    "v_mul_f32 v46, 0xbfb8aa3b, v40\n"
    "v_mul_f32 v47, 0xbfb8aa3b, v41\n"
    "v_exp_f32 v46, v46\n"
    "v_exp_f32 v47, v47\n"
    "v_mov_b32_dpp v42, v40 row_ror:8 row_mask:0xf bank_mask:0xf\n"
    "v_add_f32 v46, 1.0, v46\n"
    "v_add_f32 v47, 1.0, v47\n"
    "v_rcp_f32 v46, v46\n"
    "v_rcp_f32 v47, v47\n"
    "v_mov_b32_dpp v43, v41 row_ror:8 row_mask:0xf bank_mask:0xf\n"
    "v_pk_mul_f32 v[40:41], v[40:41], v[46:47]\n"
    "v_cvt_pk_bf16_f32 v44, v40, v41\n"
    "v_lshlrev_b32 v40, 16, v44\n"
    "v_and_b32 v41, 0xffff0000, v44\n"
    "v_pk_mul_f32 v[40:41], v[40:41], v[42:43]\n"
    "v_cvt_pk_bf16_f32 v44, v40, v41\n"
    "s_nop 1\n"
    "v_mov_b32_dpp v152, v44 quad_perm:[0,0,0,0] row_mask:0xf bank_mask:0xf\n"
    "v_mov_b32_dpp v153, v44 quad_perm:[1,1,1,1] row_mask:0xf bank_mask:0xf\n"
    "v_mov_b32_dpp v154, v44 quad_perm:[2,2,2,2] row_mask:0xf bank_mask:0xf\n"
    "v_mov_b32_dpp v155, v44 quad_perm:[3,3,3,3] row_mask:0xf bank_mask:0xf\n"
    "s_nop 1\n"
    "v_mfma_f32_16x16x32_bf16 v[120:123], v[152:155], v[88:91], 0\n"
    "v_mfma_f32_16x16x32_bf16 v[124:127], v[152:155], v[92:95], 0\n"
    "v_mfma_f32_16x16x32_bf16 v[128:131], v[152:155], v[96:99], 0\n"
    "v_mfma_f32_16x16x32_bf16 v[132:135], v[152:155], v[100:103], 0\n"
    "s_mov_b32 exec_hi, 0\n"
    "s_nop 3\n"
    "v_and_or_b32 v48, v120, s50, v167\n"
    "v_and_or_b32 v49, v124, s50, v167\n"
    "v_and_or_b32 v50, v128, s50, v167\n"
    "v_and_or_b32 v51, v132, s50, v167\n"
    "buffer_store_dwordx4 v[48:51], v159, s[32:35], 0 offen sc1\n"
    "s_endpgm\n"
    ".Lfk_fin:\n"
    "s_mul_i32 s53, s30, 14\n"
    "s_add_u32 s53, s53, s31\n"
    "s_lshl_b32 s53, s53, 11\n"
    "s_lshr_b32 s55, s25, 0\n"
    "s_mul_i32 s55, s55, 57344\n"
    "s_add_u32 s53, s53, s55\n"
    "s_add_u32 s58, s53, 851968\n"
    "v_lshlrev_b32 v11, 8, v1\n"
    "s_lshr_b32 s53, s29, 1\n"
    "s_cmp_ge_u32 s53, 14\n"
    "s_cselect_b32 s54, 1, 0\n"
    "s_mul_i32 s55, s54, 14\n"
    "s_sub_u32 s53, s53, s55\n"
    "s_lshl1_add_u32 s55, s30, s54\n"
    "s_mul_i32 s56, s55, 100352\n"
    "s_lshl_b32 s57, s53, 7\n"
    "s_add_u32 s56, s56, s57\n"
    "s_add_u32 s56, s56, 1310720\n"
    "v_lshrrev_b32 v8, 3, v1\n"
    "v_and_b32 v12, 3, v8\n"
    "v_mul_u32_u24 v8, 1792, v8\n"
    "v_and_b32 v9, 7, v1\n"
    "v_lshl_add_u32 v8, v9, 4, v8\n"
    "v_add_u32 v8, s56, v8\n"
    "s_mul_i32 s54, s54, 896\n"
    "s_lshl_b32 s57, s53, 6\n"
    "s_add_u32 s54, s54, s57\n"
    "v_lshlrev_b32 v9, 3, v9\n"
    "v_lshl_add_u32 v9, v12, 1, v9\n"
    "v_add_u32 v12, s54, v9\n"
    "s_mul_i32 s55, s55, 896\n"
    "s_add_u32 s55, s55, s57\n"
    "v_add_u32 v9, s55, v9\n"
    "s_mov_b32 s36, s20\n"
    "s_and_b32 s37, s21, 0xffff\n"
    "s_mov_b32 s38, 3584\n"
    "s_mov_b32 s39, 0x20000\n"
    "s_mov_b32 s63, 0x400000\n"
    ".Lfk_sp82:\n"
    "ds_read_b32 v10, v0 offset:28\n"
    "s_sub_u32 s63, s63, 1\n"
    "s_cbranch_scc1 .Lfk_so83\n"
    "s_waitcnt lgkmcnt(0)\n"
    "v_cmp_gt_u32 vcc, 4, v10\n"
    "s_cbranch_vccz .Lfk_so83\n"
    "s_sleep 2\n"
    "s_branch .Lfk_sp82\n"
    ".Lfk_so83:\n"
    "s_waitcnt lgkmcnt(0)\n"
    "ds_read_b32 v13, v0 offset:64\n"
    "s_waitcnt lgkmcnt(0)\n"
    "v_readfirstlane_b32 s49, v13\n"
    "s_nop 0\n"
    "s_and_b32 s48, s49, 15\n"
    "s_sleep 40\n"
    "s_mov_b64 exec, 0xff\n"
    "s_mov_b32 s62, 0\n"
    ".Lfk_scout:\n"
    "buffer_load_dwordx4 v[16:19], v11, s[32:35], s58 offen sc1\n"
    "s_add_u32 s62, s62, 1\n"
    "s_waitcnt vmcnt(0)\n"
    "v_xor_b32 v16, s48, v16\n"
    "v_xor_b32 v17, s48, v17\n"
    "v_xor_b32 v18, s48, v18\n"
    "v_xor_b32 v19, s48, v19\n"
    "v_or3_b32 v16, v16, v17, v18\n"
    "v_bitop3_b32 v16, v16, v19, 15 bitop3:0xa8\n"
    "v_cmp_eq_u32 vcc, 0, v16\n"
    "s_nop 0\n"
    "s_bcnt1_i32_b64 s53, vcc\n"
    "s_cmp_ge_u32 s53, 6\n"
    "s_cbranch_scc1 .Lfk_scouted\n"
    "s_sleep 1\n"
    "s_cmp_lt_u32 s62, 0x20000\n"
    "s_cbranch_scc1 .Lfk_scout\n"
    ".Lfk_scouted:\n"
    "s_mov_b64 exec, -1\n"
    "s_barrier\n"
    "s_bitcmp1_b32 s29, 0\n"
    "s_cbranch_scc1 .Lfk_done\n"
    "v_mov_b32 v13, 0\n"
    "v_mov_b32 v14, s49\n"
    "s_barrier\n"
    "ds_read_u16_d16_hi v13, v12 offset:5888\n"
    "s_sleep 30\n"
    "s_mov_b32 s54, 14336\n"
    "s_mov_b32 s55, 28672\n"
    "s_mov_b32 s56, 43008\n"
    "s_mov_b32 s57, 57344\n"
    "s_mov_b32 s58, 71680\n"
    "s_mov_b32 s59, 86016\n"
    "buffer_load_dwordx4 v[16:19], v8, s[32:35], 0 offen sc1\n"
    "buffer_load_dwordx4 v[20:23], v8, s[32:35], s54 offen sc1\n"
    "buffer_load_dwordx4 v[24:27], v8, s[32:35], s55 offen sc1\n"
    "buffer_load_dwordx4 v[28:31], v8, s[32:35], s56 offen sc1\n"
    "buffer_load_dwordx4 v[32:35], v8, s[32:35], s57 offen sc1\n"
    "buffer_load_dwordx4 v[36:39], v8, s[32:35], s58 offen sc1\n"
    "buffer_load_dwordx4 v[40:43], v8, s[32:35], s59 offen sc1\n"
    "s_mov_b32 s62, 0\n"
    "s_waitcnt vmcnt(6)\n"
    "v_xor_b32 v16, s48, v16\n"
    "v_xor_b32 v17, s48, v17\n"
    "v_xor_b32 v18, s48, v18\n"
    "v_xor_b32 v19, s48, v19\n"
    "v_or3_b32 v15, v16, v17, v18\n"
    "v_bitop3_b32 v15, v15, v19, 15 bitop3:0xa8\n"
    "v_cmp_ne_u32 s[4:5], 0, v15\n"
    "s_waitcnt vmcnt(5)\n"
    "v_xor_b32 v20, s48, v20\n"
    "v_xor_b32 v21, s48, v21\n"
    "v_xor_b32 v22, s48, v22\n"
    "v_xor_b32 v23, s48, v23\n"
    "v_or3_b32 v15, v20, v21, v22\n"
    "v_bitop3_b32 v15, v15, v23, 15 bitop3:0xa8\n"
    "v_cmp_ne_u32 s[6:7], 0, v15\n"
    "s_waitcnt vmcnt(4)\n"
    "v_xor_b32 v24, s48, v24\n"
    "v_xor_b32 v25, s48, v25\n"
    "v_xor_b32 v26, s48, v26\n"
    "v_xor_b32 v27, s48, v27\n"
    "v_or3_b32 v15, v24, v25, v26\n"
    "v_bitop3_b32 v15, v15, v27, 15 bitop3:0xa8\n"
    "v_cmp_ne_u32 s[8:9], 0, v15\n"
    "s_waitcnt vmcnt(3)\n"
    "v_xor_b32 v28, s48, v28\n"
    "v_xor_b32 v29, s48, v29\n"
    "v_xor_b32 v30, s48, v30\n"
    "v_xor_b32 v31, s48, v31\n"
    "v_or3_b32 v15, v28, v29, v30\n"
    "v_bitop3_b32 v15, v15, v31, 15 bitop3:0xa8\n"
    "v_cmp_ne_u32 s[10:11], 0, v15\n"
    "s_waitcnt vmcnt(2)\n"
    "v_xor_b32 v32, s48, v32\n"
    "v_xor_b32 v33, s48, v33\n"
    "v_xor_b32 v34, s48, v34\n"
    "v_xor_b32 v35, s48, v35\n"
    "v_or3_b32 v15, v32, v33, v34\n"
    "v_bitop3_b32 v15, v15, v35, 15 bitop3:0xa8\n"
    "v_cmp_ne_u32 s[12:13], 0, v15\n"
    "s_waitcnt vmcnt(1)\n"
    "v_xor_b32 v36, s48, v36\n"
    "v_xor_b32 v37, s48, v37\n"
    "v_xor_b32 v38, s48, v38\n"
    "v_xor_b32 v39, s48, v39\n"
    "v_or3_b32 v15, v36, v37, v38\n"
    "v_bitop3_b32 v15, v15, v39, 15 bitop3:0xa8\n"
    "v_cmp_ne_u32 s[14:15], 0, v15\n"
    "s_waitcnt vmcnt(0)\n"
    "v_xor_b32 v40, s48, v40\n"
    "v_xor_b32 v41, s48, v41\n"
    "v_xor_b32 v42, s48, v42\n"
    "v_xor_b32 v43, s48, v43\n"
    "v_or3_b32 v15, v40, v41, v42\n"
    "v_bitop3_b32 v15, v15, v43, 15 bitop3:0xa8\n"
    "v_cmp_ne_u32 s[16:17], 0, v15\n"
    "s_or_b64 vcc, s[4:5], s[6:7]\n"
    "s_or_b64 vcc, vcc, s[8:9]\n"
    "s_or_b64 vcc, vcc, s[10:11]\n"
    "s_or_b64 vcc, vcc, s[12:13]\n"
    "s_or_b64 vcc, vcc, s[14:15]\n"
    "s_or_b64 vcc, vcc, s[16:17]\n"
    "s_cbranch_scc0 .Lfk_go85\n"
    ".Lfk_g84:\n"
    "s_cmp_eq_u64 s[4:5], 0\n"
    "s_cbranch_scc1 .Lfk_s86\n"
    "buffer_load_dwordx4 v[16:19], v8, s[32:35], 0 offen sc1\n"
    ".Lfk_s86:\n"
    "s_cmp_eq_u64 s[6:7], 0\n"
    "s_cbranch_scc1 .Lfk_s87\n"
    "buffer_load_dwordx4 v[20:23], v8, s[32:35], s54 offen sc1\n"
    ".Lfk_s87:\n"
    "s_cmp_eq_u64 s[8:9], 0\n"
    "s_cbranch_scc1 .Lfk_s88\n"
    "buffer_load_dwordx4 v[24:27], v8, s[32:35], s55 offen sc1\n"
    ".Lfk_s88:\n"
    "s_cmp_eq_u64 s[10:11], 0\n"
    "s_cbranch_scc1 .Lfk_s89\n"
    "buffer_load_dwordx4 v[28:31], v8, s[32:35], s56 offen sc1\n"
    ".Lfk_s89:\n"
    "s_cmp_eq_u64 s[12:13], 0\n"
    "s_cbranch_scc1 .Lfk_s90\n"
    "buffer_load_dwordx4 v[32:35], v8, s[32:35], s57 offen sc1\n"
    ".Lfk_s90:\n"
    "s_cmp_eq_u64 s[14:15], 0\n"
    "s_cbranch_scc1 .Lfk_s91\n"
    "buffer_load_dwordx4 v[36:39], v8, s[32:35], s58 offen sc1\n"
    ".Lfk_s91:\n"
    "s_cmp_eq_u64 s[16:17], 0\n"
    "s_cbranch_scc1 .Lfk_s92\n"
    "buffer_load_dwordx4 v[40:43], v8, s[32:35], s59 offen sc1\n"
    ".Lfk_s92:\n"
    "s_add_u32 s62, s62, 1\n"
    "s_waitcnt vmcnt(0)\n"
    "s_cmp_eq_u64 s[4:5], 0\n"
    "s_cbranch_scc1 .Lfk_s93\n"
    "v_xor_b32 v16, s48, v16\n"
    "v_xor_b32 v17, s48, v17\n"
    "v_xor_b32 v18, s48, v18\n"
    "v_xor_b32 v19, s48, v19\n"
    "v_or3_b32 v15, v16, v17, v18\n"
    "v_bitop3_b32 v15, v15, v19, 15 bitop3:0xa8\n"
    "v_cmp_ne_u32 s[4:5], 0, v15\n"
    ".Lfk_s93:\n"
    "s_cmp_eq_u64 s[6:7], 0\n"
    "s_cbranch_scc1 .Lfk_s94\n"
    "v_xor_b32 v20, s48, v20\n"
    "v_xor_b32 v21, s48, v21\n"
    "v_xor_b32 v22, s48, v22\n"
    "v_xor_b32 v23, s48, v23\n"
    "v_or3_b32 v15, v20, v21, v22\n"
    "v_bitop3_b32 v15, v15, v23, 15 bitop3:0xa8\n"
    "v_cmp_ne_u32 s[6:7], 0, v15\n"
    ".Lfk_s94:\n"
    "s_cmp_eq_u64 s[8:9], 0\n"
    "s_cbranch_scc1 .Lfk_s95\n"
    "v_xor_b32 v24, s48, v24\n"
    "v_xor_b32 v25, s48, v25\n"
    "v_xor_b32 v26, s48, v26\n"
    "v_xor_b32 v27, s48, v27\n"
    "v_or3_b32 v15, v24, v25, v26\n"
    "v_bitop3_b32 v15, v15, v27, 15 bitop3:0xa8\n"
    "v_cmp_ne_u32 s[8:9], 0, v15\n"
    ".Lfk_s95:\n"
    "s_cmp_eq_u64 s[10:11], 0\n"
    "s_cbranch_scc1 .Lfk_s96\n"
    "v_xor_b32 v28, s48, v28\n"
    "v_xor_b32 v29, s48, v29\n"
    "v_xor_b32 v30, s48, v30\n"
    "v_xor_b32 v31, s48, v31\n"
    "v_or3_b32 v15, v28, v29, v30\n"
    "v_bitop3_b32 v15, v15, v31, 15 bitop3:0xa8\n"
    "v_cmp_ne_u32 s[10:11], 0, v15\n"
    ".Lfk_s96:\n"
    "s_cmp_eq_u64 s[12:13], 0\n"
    "s_cbranch_scc1 .Lfk_s97\n"
    "v_xor_b32 v32, s48, v32\n"
    "v_xor_b32 v33, s48, v33\n"
    "v_xor_b32 v34, s48, v34\n"
    "v_xor_b32 v35, s48, v35\n"
    "v_or3_b32 v15, v32, v33, v34\n"
    "v_bitop3_b32 v15, v15, v35, 15 bitop3:0xa8\n"
    "v_cmp_ne_u32 s[12:13], 0, v15\n"
    ".Lfk_s97:\n"
    "s_cmp_eq_u64 s[14:15], 0\n"
    "s_cbranch_scc1 .Lfk_s98\n"
    "v_xor_b32 v36, s48, v36\n"
    "v_xor_b32 v37, s48, v37\n"
    "v_xor_b32 v38, s48, v38\n"
    "v_xor_b32 v39, s48, v39\n"
    "v_or3_b32 v15, v36, v37, v38\n"
    "v_bitop3_b32 v15, v15, v39, 15 bitop3:0xa8\n"
    "v_cmp_ne_u32 s[14:15], 0, v15\n"
    ".Lfk_s98:\n"
    "s_cmp_eq_u64 s[16:17], 0\n"
    "s_cbranch_scc1 .Lfk_s99\n"
    "v_xor_b32 v40, s48, v40\n"
    "v_xor_b32 v41, s48, v41\n"
    "v_xor_b32 v42, s48, v42\n"
    "v_xor_b32 v43, s48, v43\n"
    "v_or3_b32 v15, v40, v41, v42\n"
    "v_bitop3_b32 v15, v15, v43, 15 bitop3:0xa8\n"
    "v_cmp_ne_u32 s[16:17], 0, v15\n"
    ".Lfk_s99:\n"
    "s_or_b64 vcc, s[4:5], s[6:7]\n"
    "s_or_b64 vcc, vcc, s[8:9]\n"
    "s_or_b64 vcc, vcc, s[10:11]\n"
    "s_or_b64 vcc, vcc, s[12:13]\n"
    "s_or_b64 vcc, vcc, s[14:15]\n"
    "s_or_b64 vcc, vcc, s[16:17]\n"
    "s_cbranch_scc0 .Lfk_go85\n"
    "s_cmp_lt_u32 s62, 0x20000\n"
    "s_cbranch_scc1 .Lfk_g84\n"
    ".Lfk_go85:\n"
    "v_pk_add_f32 v[16:17], v[16:17], v[20:21]\n"
    "v_pk_add_f32 v[18:19], v[18:19], v[22:23]\n"
    "v_pk_add_f32 v[24:25], v[24:25], v[28:29]\n"
    "v_pk_add_f32 v[26:27], v[26:27], v[30:31]\n"
    "v_pk_add_f32 v[32:33], v[32:33], v[36:37]\n"
    "v_pk_add_f32 v[34:35], v[34:35], v[38:39]\n"
    "v_pk_add_f32 v[16:17], v[16:17], v[24:25]\n"
    "v_pk_add_f32 v[18:19], v[18:19], v[26:27]\n"
    "v_pk_add_f32 v[32:33], v[32:33], v[40:41]\n"
    "v_pk_add_f32 v[34:35], v[34:35], v[42:43]\n"
    "v_pk_add_f32 v[44:45], v[16:17], v[32:33]\n"
    "v_pk_add_f32 v[46:47], v[18:19], v[34:35]\n"
    "s_nop 0\n"
    "v_add_f32_dpp v48, v44, v44 row_ror:8 row_mask:0xf bank_mask:0x3\n"
    "v_add_f32_dpp v49, v46, v46 row_ror:8 row_mask:0xf bank_mask:0x3\n"
    "s_nop 0\n"
    "v_add_f32_dpp v48, v45, v45 row_ror:8 row_mask:0xf bank_mask:0xc\n"
    "v_add_f32_dpp v49, v47, v47 row_ror:8 row_mask:0xf bank_mask:0xc\n"
    "s_nop 1\n"
    "v_permlane16_swap_b32 v48, v49\n"
    "v_add_f32 v48, v48, v49\n"
    "v_mov_b32 v49, v48\n"
    "s_nop 1\n"
    "v_permlane32_swap_b32 v48, v49\n"
    "v_add_f32 v48, v48, v49\n"
    "v_cvt_pk_bf16_f32 v48, v48, v48\n"
    "v_lshlrev_b32 v48, 16, v48\n"
    "s_waitcnt lgkmcnt(0)\n"
    "v_add_f32 v48, v48, v13\n"
    "v_cvt_pk_bf16_f32 v48, v48, v48\n"
    "s_mov_b32 exec_hi, 0\n"
    "buffer_store_short v48, v9, s[36:39], 0 offen\n"
    "s_or_b32 s53, s29, s30\n"
    "s_cbranch_scc1 .Lfk_fin_done\n"
    "s_mov_b64 exec, 1\n"
    "buffer_store_dword v14, off, s[32:35], 0 sc1\n"
    ".Lfk_fin_done:\n"
    "s_endpgm\n"
    ".Lfk_end:\n"
    ".if (.Lfk_end - .Lfk_top) < 13400\n"
    ".fill (13400 - (.Lfk_end - .Lfk_top)) / 4, 4, 0xbf800000\n"
    ".endif\n"
    :
    : "{s[0:1]}"(__builtin_amdgcn_kernarg_segment_ptr()), "{s2}"(__builtin_amdgcn_workgroup_id_x()), "{v0}"(__builtin_amdgcn_workitem_id_x()),
      "i"((__attribute__((address_space(3))) char*)fk_smem)
    : "v167", "s95", "vcc", "memory");
}

extern "C" void kernel_launch(void* const* d_in, const int* in_sizes, int n_in, void* d_out, int out_size, void* d_ws, size_t ws_size, hipStream_t stream) {
  (void)in_sizes; (void)n_in; (void)out_size; (void)ws_size;
  fused_block<<<256, 768, 0, stream>>>(d_in[0], d_in[1], d_in[2], d_in[3], d_in[4], d_in[6], d_in[7], d_in[8], d_out, d_ws);
}
```
